```python
import torch

def _softmax(x, axis=-1):
    return torch.softmax(x, dim=axis)
def _gelu(x):
    return torch.nn.functional.gelu(x, approximate="tanh")
def _silu(x):
    return torch.nn.functional.silu(x)
def _sigmoid(x):
    return torch.sigmoid(x)
def _layernorm(x, axis=-1, eps=1e-5):
    m = x.mean(dim=axis, keepdim=True)
    v = x.var(dim=axis, keepdim=True, unbiased=False)
    return (x - m) / torch.sqrt(v + eps)
def _scatter_add(tgt, idx, src, axis=0):
    return tgt.clone().index_add_(axis, idx, src)
def _segment_sum(src, seg, n):
    out = torch.zeros((n,) + src.shape[1:], dtype=src.dtype)
    return out.index_add_(0, seg, src)
def _pad(x, pw):
    flat = tuple(v for pair in reversed(pw) for v in pair)
    return torch.nn.functional.pad(x, flat)

kernel_name = 'concat_heads__mha_block_cached__gqa_block_cached__near_ridge'

def setup_inputs():

    _w = (lambda a: (lambda t: t.to(torch.bfloat16) if t.is_floating_point() else t)(torch.as_tensor(a)))
    def _A_setup():
        B, Sq, D = 8, 144, 640
        H, Sk, Dh = 32, 512, 128
        return {
            "x": _w((lambda *s: torch.randn(*s, dtype=torch.bfloat16) * 0.8)(B, Sq, D)),
            "k_cache": _w((lambda *s: torch.randn(*s, dtype=torch.bfloat16) * 0.8)(B, H, Sk, Dh)),
            "v_cache": _w((lambda *s: torch.randn(*s, dtype=torch.bfloat16) * 0.05)(B, H, Sk, Dh)),
            "wq": _w((lambda *s: torch.randn(*s, dtype=torch.bfloat16) * 0.8)(D, H * Dh) * (D ** -0.5)),
            "wo": _w((lambda *s: torch.randn(*s, dtype=torch.bfloat16) * 0.05)(H * Dh, D) * ((H * Dh) ** -0.5)),
        }
    _A_in = _A_setup()

    def _B_setup():
        B, Sq, D = 8, 144, 640
        Hq, Hkv, Sk, Dh = 32, 32, 512, 128
        assert Hq % Hkv == 0, (Hq, Hkv)
        return {
            "x": _w((lambda *s: torch.randn(*s, dtype=torch.bfloat16) * 0.8)(B, Sq, D)),
            "k_cache": _w((lambda *s: torch.randn(*s, dtype=torch.bfloat16) * 0.8)(B, Hkv, Sk, Dh)),
            "v_cache": _w((lambda *s: torch.randn(*s, dtype=torch.bfloat16) * 0.05)(B, Hkv, Sk, Dh)),
            "wq": _w((lambda *s: torch.randn(*s, dtype=torch.bfloat16) * 0.8)(D, Hq * Dh) * (D ** -0.5)),
            "wo": _w((lambda *s: torch.randn(*s, dtype=torch.bfloat16) * 0.05)(Hq * Dh, D) * ((Hq * Dh) ** -0.5)),
        }
    _B_in = _B_setup()

    return {
        "A_x": _A_in["x"],
        "A_k_cache": _A_in["k_cache"],
        "A_v_cache": _A_in["v_cache"],
        "A_wq": _A_in["wq"],
        "A_wo": _A_in["wo"],
        "B_x": _B_in["x"],
        "B_k_cache": _B_in["k_cache"],
        "B_v_cache": _B_in["v_cache"],
        "B_wq": _B_in["wq"],
        "B_wo": _B_in["wo"],
    }

def reference(A_x, A_k_cache, A_v_cache, A_wq, A_wo, B_x, B_k_cache, B_v_cache, B_wq, B_wo):
    def _A_ref(x, k_cache, v_cache, wq, wo):
        B, Sq, D = x.shape
        H, Dh = 32, 128
        q = (lambda x, *d: x.permute(*d))((x @ wq).reshape(B, Sq, H, Dh), 0, 2, 1, 3)
        scores = torch.einsum("bhqd,bhkd->bhqk", q, k_cache) * (Dh ** -0.5)
        p = _softmax(scores, axis=-1)
        o = torch.einsum("bhqk,bhkd->bhqd", p, v_cache)
        return (lambda x, *d: x.permute(*d))(o, 0, 2, 1, 3).reshape(B, Sq, H * Dh) @ wo

    def _B_ref(x, k_cache, v_cache, wq, wo):
        B, Sq, D = x.shape
        Hq, Dh = 32, 128
        Hkv = k_cache.shape[1]
        rep = Hq // Hkv
        q = (lambda x, *d: x.permute(*d))((x @ wq).reshape(B, Sq, Hq, Dh), 0, 2, 1, 3)
        k = (lambda x, n, axis: torch.repeat_interleave(x, n, dim=axis))(k_cache, rep, axis=1)
        v = (lambda x, n, axis: torch.repeat_interleave(x, n, dim=axis))(v_cache, rep, axis=1)
        scores = torch.einsum("bhqd,bhkd->bhqk", q, k) * (Dh ** -0.5)
        p = _softmax(scores, axis=-1)
        o = torch.einsum("bhqk,bhkd->bhqd", p, v)
        return (lambda x, *d: x.permute(*d))(o, 0, 2, 1, 3).reshape(B, Sq, Hq * Dh) @ wo

    a = _A_ref(A_x, A_k_cache, A_v_cache, A_wq, A_wo)
    b = _B_ref(B_x, B_k_cache, B_v_cache, B_wq, B_wo)
    return torch.concatenate([a, b], axis=-1)
```

```cpp
#include <hip/hip_runtime.h>
#include <hip/hip_bf16.h>
#include <stdint.h>

constexpr int NB = 8, SQ = 144, DM = 640, NH = 32, SK = 512, DH = 128;
constexpr int NQ = NH * DH;
constexpr int MROWS = NB * SQ;

typedef short s16x4 __attribute__((ext_vector_type(4)));
typedef short s16x8 __attribute__((ext_vector_type(8)));
typedef __bf16 bf16x8 __attribute__((ext_vector_type(8)));
typedef __bf16 bf16x2 __attribute__((ext_vector_type(2)));
typedef float f32x4 __attribute__((ext_vector_type(4)));
typedef float f32x2 __attribute__((ext_vector_type(2)));
typedef unsigned u32x4 __attribute__((ext_vector_type(4)));
typedef unsigned u32x2 __attribute__((ext_vector_type(2)));
#define LAS __attribute__((address_space(3)))
#define DI __device__ __forceinline__

template <int N> DI void waitv() { asm volatile("s_waitcnt vmcnt(%0)" ::"i"(N) : "memory"); }
DI void waitl0() { asm volatile("s_waitcnt lgkmcnt(0)" ::: "memory"); }
DI void bar() {
  asm volatile("s_waitcnt lgkmcnt(0)" ::: "memory");
  __builtin_amdgcn_s_barrier();
  asm volatile("" ::: "memory");
}
DI void glds(unsigned voff, const void* sbase, unsigned lds) {
  unsigned keep;
  asm volatile("s_mov_b32 %0, m0\n\ts_mov_b32 m0, %3\n\ts_nop 0\n\tglobal_load_lds_dwordx4 %1, %2\n\ts_mov_b32 m0, %0"
               : "=&s"(keep) : "v"(voff), "s"(sbase), "s"(lds) : "memory");
}
DI u32x4 gload16(unsigned voff, const void* sbase) {
  u32x4 r;
  asm volatile("global_load_dwordx4 %0, %1, %2" : "=v"(r) : "v"(voff), "s"(sbase) : "memory");
  return r;
}
DI bf16x8 lds_b128(unsigned a) { return __builtin_bit_cast(bf16x8, *(const LAS s16x8*)(uintptr_t)a); }
DI bf16x8 lds_tr2(unsigned a_lo, unsigned a_hi) {
  s16x4 lo = __builtin_amdgcn_ds_read_tr16_b64_v4i16((LAS s16x4*)(uintptr_t)a_lo);
  s16x4 hi = __builtin_amdgcn_ds_read_tr16_b64_v4i16((LAS s16x4*)(uintptr_t)a_hi);
  return __builtin_bit_cast(bf16x8, __builtin_shufflevector(lo, hi, 0, 1, 2, 3, 4, 5, 6, 7));
}
DI f32x4 mfma(bf16x8 a, bf16x8 b, f32x4 c) { return __builtin_amdgcn_mfma_f32_16x16x32_bf16(a, b, c, 0, 0, 0); }
DI unsigned pk2(float a, float b) {
  f32x2 v = {a, b};
  return __builtin_bit_cast(unsigned, __builtin_convertvector(v, bf16x2));
}
DI float xmax16_32(float v) {
  auto r = __builtin_amdgcn_permlane16_swap(__float_as_uint(v), __float_as_uint(v), false, false);
  v = fmaxf(__uint_as_float(r[0]), __uint_as_float(r[1]));
  auto s = __builtin_amdgcn_permlane32_swap(__float_as_uint(v), __float_as_uint(v), false, false);
  return fmaxf(__uint_as_float(s[0]), __uint_as_float(s[1]));
}
DI float xsum16_32(float v) {
  auto r = __builtin_amdgcn_permlane16_swap(__float_as_uint(v), __float_as_uint(v), false, false);
  v = __uint_as_float(r[0]) + __uint_as_float(r[1]);
  auto s = __builtin_amdgcn_permlane32_swap(__float_as_uint(v), __float_as_uint(v), false, false);
  return __uint_as_float(s[0]) + __uint_as_float(s[1]);
}

constexpr float SM_C = 0.088388347648318440f * 1.4426950408889634f;

constexpr int SLOT = 16384;
constexpr int K1_LDS = 5 * SLOT;

struct AttnPtrs {
  const char* x[2];
  const char* k[2];
  const char* v[2];
  const char* wq[2];
  const char* wo[2];
  char* o;
  char* out;
};

__global__ __launch_bounds__(256, 2) void k_attn(AttnPtrs P) {
  extern __shared__ __attribute__((aligned(16))) char smem[];
  const unsigned L0 = (unsigned)(uintptr_t)smem;
  const int tid = threadIdx.x, lane = tid & 63;
  const int w = __builtin_amdgcn_readfirstlane(tid >> 6);
  const int li = lane & 15, g = lane >> 4;
  const int blk = blockIdx.x;
  const int p = blk >> 8, idx = blk & 255, xcd = idx & 7, j = idx >> 3;
  const int h = 8 * (xcd & 3) + (j & 7), b = 4 * (xcd >> 2) + (j >> 3);

  const char* xb = P.x[p] + (size_t)b * SQ * DM * 2;
  const char* wqh = P.wq[p] + h * 256;
  const char* Kp = P.k[p] + (size_t)(b * NH + h) * SK * 256;
  const char* Vp = P.v[p] + (size_t)(b * NH + h) * SK * 256;

  const int q4 = (lane & 15) >> 2, p4 = lane & 3;
  const unsigned tr0 = (unsigned)((8 * g + q4) * 256 + 8 * p4 + ((q4 | ((g & 1) << 2)) << 5));
  const int r4 = lane >> 4, c16 = lane & 15;
  const unsigned fV = (unsigned)(r4 | ((w & 1) << 2));
  const unsigned lcV = (((c16 >> 1) ^ fV) << 1) | (c16 & 1);
  const unsigned voffV = (unsigned)((8 * w + r4) * 256) + lcV * 16;
  const unsigned voffW = (unsigned)((8 * w + r4) * 8192) + lcV * 16;
  const unsigned fK = (unsigned)((w << 2) | r4);
  const unsigned voffK = (unsigned)((8 * w + r4) * 256) + ((c16 ^ fK) * 16);
  const unsigned krow = (unsigned)(8 * (li >> 2) + (li & 3));
  const unsigned voffX0 = (unsigned)((32 * w + li) * 1280 + g * 16);
  const unsigned voffX1 = voffX0 + 16 * 1280;
  const unsigned voffXS = (unsigned)((128 + li) * 1280 + g * 16);

  auto issue_tile = [&](int tile, unsigned slot_off) {
    const char* src = ((tile & 1) ? Vp : Kp) + (size_t)(tile >> 1) * SLOT;
    const unsigned vo = (tile & 1) ? voffV : voffK;
    const unsigned d = L0 + slot_off + w * 2048;
    glds(vo, src, d);
    glds(vo, src + 1024, d + 1024);
    glds(vo, src + 8192, d + 8192);
    glds(vo, src + 9216, d + 9216);
  };

  const unsigned WR = L0 + 3 * SLOT;
  f32x4 qa0[8], qa1[8], qaS[2];
#pragma unroll
  for (int i = 0; i < 8; ++i) { qa0[i] = f32x4{0, 0, 0, 0}; qa1[i] = f32x4{0, 0, 0, 0}; }
  qaS[0] = f32x4{0, 0, 0, 0}; qaS[1] = f32x4{0, 0, 0, 0};
  unsigned ta[8];
#pragma unroll
  for (int i = 0; i < 8; ++i) ta[i] = WR + (tr0 ^ (unsigned)(((i + 2 * w) & 7) << 5));

  u32x4 xr0[4], xr1[4], xrS[4];
  auto issue_q = [&](int s) {
    const char* ws = wqh + (size_t)s * 32 * 8192;
    const unsigned d = WR + (s & 3) * 8192 + w * 2048;
    glds(voffW, ws, d);
    glds(voffW, ws + 4 * 8192, d + 1024);
    const char* xs = xb + s * 64;
    xr0[s & 3] = gload16(voffX0, xs);
    xr1[s & 3] = gload16(voffX1, xs);
    xrS[s & 3] = gload16(voffXS, xs);
  };
  issue_q(0); issue_q(1); issue_q(2);
#pragma unroll
  for (int s = 0; s < 20; ++s) {
    if (s <= 17) asm volatile("s_waitcnt vmcnt(10)" : "+v"(xr0[s & 3]), "+v"(xr1[s & 3]), "+v"(xrS[s & 3])::"memory");
    else if (s == 18) asm volatile("s_waitcnt vmcnt(9)" : "+v"(xr0[s & 3]), "+v"(xr1[s & 3]), "+v"(xrS[s & 3])::"memory");
    else asm volatile("s_waitcnt vmcnt(8)" : "+v"(xr0[s & 3]), "+v"(xr1[s & 3]), "+v"(xrS[s & 3])::"memory");
    bar();
    if (s + 3 < 20) issue_q(s + 3);
    else issue_tile(s - 17, (s - 17) * SLOT);
    const bf16x8 b0 = __builtin_bit_cast(bf16x8, xr0[s & 3]);
    const bf16x8 b1 = __builtin_bit_cast(bf16x8, xr1[s & 3]);
    const bf16x8 bS = __builtin_bit_cast(bf16x8, xrS[s & 3]);
#pragma unroll
    for (int i = 0; i < 8; ++i) {
      const unsigned a = ta[i] + (s & 3) * 8192;
      const bf16x8 af = lds_tr2(a, a + 1024);
      qa0[i] = mfma(af, b0, qa0[i]);
      qa1[i] = mfma(af, b1, qa1[i]);
      if (i < 2) qaS[i] = mfma(af, bS, qaS[i]);
    }
  }
  bf16x8 Q0[4], Q1[4], QS[4];
  {
    bar();
    const unsigned own = L0 + 3 * SLOT + w * 4096, shr = L0 + 4 * SLOT;
    auto waddr = [&](unsigned base, int T) { return base + li * 256 + ((((2 * T + (g >> 1)) ^ li) & 15) << 4) + 8 * (g & 1); };
    auto raddr = [&](unsigned base, int ds) { return base + li * 256 + ((((4 * ds + g) ^ li) & 15) << 4); };
#pragma unroll
    for (int t = 0; t < 2; ++t) {
      u32x2 v = {pk2(qaS[t][0], qaS[t][1]), pk2(qaS[t][2], qaS[t][3])};
      *(LAS u32x2*)(uintptr_t)waddr(shr, 2 * w + t) = v;
    }
#pragma unroll
    for (int i = 0; i < 8; ++i) {
      u32x2 v = {pk2(qa0[i][0], qa0[i][1]), pk2(qa0[i][2], qa0[i][3])};
      *(LAS u32x2*)(uintptr_t)waddr(own, (i + 2 * w) & 7) = v;
    }
    waitl0();
#pragma unroll
    for (int ds = 0; ds < 4; ++ds) Q0[ds] = lds_b128(raddr(own, ds));
    waitl0();
#pragma unroll
    for (int i = 0; i < 8; ++i) {
      u32x2 v = {pk2(qa1[i][0], qa1[i][1]), pk2(qa1[i][2], qa1[i][3])};
      *(LAS u32x2*)(uintptr_t)waddr(own, (i + 2 * w) & 7) = v;
    }
    waitl0();
#pragma unroll
    for (int ds = 0; ds < 4; ++ds) Q1[ds] = lds_b128(raddr(own, ds));
    bar();
#pragma unroll
    for (int ds = 0; ds < 4; ++ds) QS[ds] = lds_b128(raddr(shr, ds));
    bar();
    issue_tile(3, 3 * SLOT);
  }

  f32x4 O0[8], O1[8], OS[8];
#pragma unroll
  for (int i = 0; i < 8; ++i) { O0[i] = f32x4{0, 0, 0, 0}; O1[i] = f32x4{0, 0, 0, 0}; OS[i] = f32x4{0, 0, 0, 0}; }
  float m0 = -1e30f, m1 = -1e30f, mS = -1e30f, l0 = 0.f, l1 = 0.f, lS = 0.f;
  unsigned ka[4];
#pragma unroll
  for (int ds = 0; ds < 4; ++ds) ka[ds] = L0 + krow * 256 + ((((unsigned)(4 * ds + g) ^ (unsigned)li) & 15) << 4);
  const unsigned va0 = L0 + tr0;

  auto softmax2 = [&](f32x4* S, float& m, float& l, f32x4* O, bf16x8* Pf) {
    float mx = fmaxf(fmaxf(S[0][0], S[0][1]), fmaxf(S[0][2], S[0][3]));
#pragma unroll
    for (int kt = 1; kt < 4; ++kt) mx = fmaxf(mx, fmaxf(fmaxf(S[kt][0], S[kt][1]), fmaxf(S[kt][2], S[kt][3])));
    mx = xmax16_32(mx);
    const float mn = fmaxf(m, mx);
    const float alpha = __builtin_amdgcn_exp2f((m - mn) * SM_C);
    m = mn;
    const float nm = -mn * SM_C;
    float ps = 0.f;
#pragma unroll
    for (int kt = 0; kt < 4; ++kt)
#pragma unroll
      for (int r = 0; r < 4; ++r) { S[kt][r] = __builtin_amdgcn_exp2f(fmaf(S[kt][r], SM_C, nm)); ps += S[kt][r]; }
    l = l * alpha + ps;
#pragma unroll
    for (int dt = 0; dt < 8; ++dt) O[dt] *= alpha;
#pragma unroll
    for (int c = 0; c < 2; ++c) {
      u32x4 u = {pk2(S[2 * c][0], S[2 * c][1]), pk2(S[2 * c][2], S[2 * c][3]), pk2(S[2 * c + 1][0], S[2 * c + 1][1]), pk2(S[2 * c + 1][2], S[2 * c + 1][3])};
      Pf[c] = __builtin_bit_cast(bf16x8, u);
    }
  };

  auto iter = [&](int it, unsigned sK, unsigned sV, unsigned sNK, auto VK, auto VV, auto ISS) {
    waitv<decltype(VK)::value>();
    bar();
    if (decltype(ISS)::value) issue_tile(2 * it + 4, sNK);
    f32x4 S0[4], S1[4];
#pragma unroll
    for (int kt = 0; kt < 4; ++kt) { S0[kt] = f32x4{0, 0, 0, 0}; S1[kt] = f32x4{0, 0, 0, 0}; }
#pragma unroll
    for (int kt = 0; kt < 4; ++kt) {
#pragma unroll
      for (int ds = 0; ds < 4; ++ds) {
        const bf16x8 a = lds_b128(ka[ds] + sK + (32 * (kt >> 1) + 4 * (kt & 1)) * 256);
        S0[kt] = mfma(a, Q0[ds], S0[kt]);
        S1[kt] = mfma(a, Q1[ds], S1[kt]);
      }
    }
    const bool shared_on = ((it & 1) == (w >> 1));
    const unsigned cS = (unsigned)(w & 1) * 8192;
    bf16x8 PS;
    if (shared_on) {
      f32x4 SS[2] = {f32x4{0, 0, 0, 0}, f32x4{0, 0, 0, 0}};
#pragma unroll
      for (int t = 0; t < 2; ++t)
#pragma unroll
        for (int ds = 0; ds < 4; ++ds) {
          const bf16x8 a = lds_b128(ka[ds] + sK + cS + t * 1024);
          SS[t] = mfma(a, QS[ds], SS[t]);
        }
      float mx = fmaxf(fmaxf(fmaxf(SS[0][0], SS[0][1]), fmaxf(SS[0][2], SS[0][3])), fmaxf(fmaxf(SS[1][0], SS[1][1]), fmaxf(SS[1][2], SS[1][3])));
      mx = xmax16_32(mx);
      const float mn = fmaxf(mS, mx);
      const float alpha = __builtin_amdgcn_exp2f((mS - mn) * SM_C);
      mS = mn;
      const float nm = -mn * SM_C;
      float ps = 0.f;
#pragma unroll
      for (int t = 0; t < 2; ++t)
#pragma unroll
        for (int r = 0; r < 4; ++r) { SS[t][r] = __builtin_amdgcn_exp2f(fmaf(SS[t][r], SM_C, nm)); ps += SS[t][r]; }
      lS = lS * alpha + ps;
#pragma unroll
      for (int dt = 0; dt < 8; ++dt) OS[dt] *= alpha;
      u32x4 u = {pk2(SS[0][0], SS[0][1]), pk2(SS[0][2], SS[0][3]), pk2(SS[1][0], SS[1][1]), pk2(SS[1][2], SS[1][3])};
      PS = __builtin_bit_cast(bf16x8, u);
    }
    bf16x8 P0[2], P1[2];
    softmax2(S0, m0, l0, O0, P0);
    softmax2(S1, m1, l1, O1, P1);
    waitv<decltype(VV)::value>();
    bar();
    if (decltype(ISS)::value) issue_tile(2 * it + 5, sK);
    const unsigned vb = va0 + sV;
#pragma unroll
    for (int dt = 0; dt < 8; ++dt) {
#pragma unroll
      for (int c = 0; c < 2; ++c) {
        const unsigned a = (vb ^ (unsigned)(dt << 5)) + c * 8192;
        const bf16x8 af = lds_tr2(a, a + 1024);
        O0[dt] = mfma(af, P0[c], O0[dt]);
        O1[dt] = mfma(af, P1[c], O1[dt]);
      }
    }
    if (shared_on) {
#pragma unroll
      for (int dt = 0; dt < 8; ++dt) {
        const unsigned a = (vb ^ (unsigned)(dt << 5)) + cS;
        const bf16x8 af = lds_tr2(a, a + 1024);
        OS[dt] = mfma(af, PS, OS[dt]);
      }
    }
  };
  using I0 = std::integral_constant<int, 0>;
  using I4 = std::integral_constant<int, 4>;
  using I8 = std::integral_constant<int, 8>;
  using I12 = std::integral_constant<int, 12>;
  using T1 = std::integral_constant<bool, true>;
  using T0 = std::integral_constant<bool, false>;
  {
    unsigned sK = 0, sV = SLOT, sNK = 4 * SLOT;
    for (int it = 0; it < 6; ++it) {
      iter(it, sK, sV, sNK, I12{}, I12{}, T1{});
      sNK = sV;
      sK += 2 * SLOT; if (sK >= 5 * SLOT) sK -= 5 * SLOT;
      sV += 2 * SLOT; if (sV >= 5 * SLOT) sV -= 5 * SLOT;
    }
    iter(6, sK, sV, sNK, I12{}, I8{}, T0{});
    sK += 2 * SLOT; if (sK >= 5 * SLOT) sK -= 5 * SLOT;
    sV += 2 * SLOT; if (sV >= 5 * SLOT) sV -= 5 * SLOT;
    iter(7, sK, sV, sNK, I4{}, I0{}, T0{});
  }

  char* ob = P.o + ((size_t)(p * MROWS + b * SQ) * NQ + h * DH) * 2;
  {
    const unsigned so = L0 + SLOT + w * 8192 + lane * 16;
#pragma unroll
    for (int dt = 0; dt < 8; ++dt) *(LAS f32x4*)(uintptr_t)(so + dt * 1024) = OS[dt];
    lS = xsum16_32(lS);
    if (g == 0) {
      f32x2 ml = {mS, lS};
      *(LAS f32x2*)(uintptr_t)(L0 + 3 * SLOT + (w * 16 + li) * 8) = ml;
    }
  }
  {
    const float i0 = 1.0f / xsum16_32(l0), i1 = 1.0f / xsum16_32(l1);
    char* r0 = ob + (size_t)(32 * w + li) * (NQ * 2) + g * 8;
    char* r1 = r0 + (size_t)16 * (NQ * 2);
#pragma unroll
    for (int dt = 0; dt < 8; ++dt) {
      u32x2 v0 = {pk2(O0[dt][0] * i0, O0[dt][1] * i0), pk2(O0[dt][2] * i0, O0[dt][3] * i0)};
      u32x2 v1 = {pk2(O1[dt][0] * i1, O1[dt][1] * i1), pk2(O1[dt][2] * i1, O1[dt][3] * i1)};
      *(u32x2*)(r0 + dt * 32) = v0;
      *(u32x2*)(r1 + dt * 32) = v1;
    }
  }
  bar();
  {
    float mm[4], ll[4];
#pragma unroll
    for (int ww = 0; ww < 4; ++ww) {
      f32x2 ml = *(const LAS f32x2*)(uintptr_t)(L0 + 3 * SLOT + (ww * 16 + li) * 8);
      mm[ww] = ml[0]; ll[ww] = ml[1];
    }
    const float M = fmaxf(fmaxf(mm[0], mm[1]), fmaxf(mm[2], mm[3]));
    float sc[4], L = 0.f;
#pragma unroll
    for (int ww = 0; ww < 4; ++ww) { sc[ww] = __builtin_amdgcn_exp2f((mm[ww] - M) * SM_C); L += sc[ww] * ll[ww]; }
    const float inv = 1.0f / L;
    char* rS = ob + (size_t)(128 + li) * (NQ * 2) + g * 8;
#pragma unroll
    for (int t = 0; t < 2; ++t) {
      const int dt = 2 * w + t;
      f32x4 acc = {0, 0, 0, 0};
#pragma unroll
      for (int ww = 0; ww < 4; ++ww) {
        const f32x4 v = *(const LAS f32x4*)(uintptr_t)(L0 + SLOT + ww * 8192 + dt * 1024 + lane * 16);
        acc += v * sc[ww];
      }
      u32x2 v = {pk2(acc[0] * inv, acc[1] * inv), pk2(acc[2] * inv, acc[3] * inv)};
      *(u32x2*)(rS + dt * 32) = v;
    }
  }
}

constexpr int OP_STAGE = 4096 + 6144;
constexpr int OP_WAVE = 4 * OP_STAGE;
constexpr int K2_LDS = 4 * OP_WAVE;

__global__ __launch_bounds__(256, 1) void k_oproj(AttnPtrs P) {
  extern __shared__ __attribute__((aligned(16))) char smem[];
  const unsigned L0 = (unsigned)(uintptr_t)smem;
  const int tid = threadIdx.x, lane = tid & 63;
  const int w = __builtin_amdgcn_readfirstlane(tid >> 6);
  const int li = lane & 15, g = lane >> 4;
  const int blk = blockIdx.x;
  const int p = blk / 120, r = blk % 120, mt0 = r / 10, nt0 = r % 10;
  const int m0 = mt0 * 96, n0 = nt0 * 64;
  const char* wo = P.wo[p] + (size_t)(w * 1024) * (DM * 2) + n0 * 2;
  const char* oo = P.o + ((size_t)(p * MROWS + m0) * NQ + w * 1024) * 2;
  const unsigned WB = L0 + w * OP_WAVE;

  const int r8 = lane >> 3, c8 = lane & 7;
  const unsigned fw0 = (unsigned)((lane >> 4) & 1);
  const unsigned voffW0 = (unsigned)(r8 * (DM * 2)) + (((((unsigned)(c8 >> 1)) ^ fw0) << 1 | (c8 & 1)) * 16);
  const unsigned voffW1 = (unsigned)(r8 * (DM * 2)) + (((((unsigned)(c8 >> 1)) ^ (fw0 | 2)) << 1 | (c8 & 1)) * 16);
  auto Ff = [](unsigned jj) { return (((jj ^ (jj >> 1)) & 1) << 1) | (jj >> 1); };
  const unsigned voffO = (unsigned)((lane >> 2) * (NQ * 2)) + (((unsigned)(lane & 3) ^ Ff((unsigned)(lane >> 4))) * 16);
  const int q4 = li >> 2, p4 = lane & 3;
  const unsigned fr = (unsigned)(((q4 >> 1) & 1) | ((g & 1) << 1));
  const unsigned ta0 = (unsigned)((8 * g + q4) * 128 + 8 * p4) + (fr << 5);
  const unsigned ba0 = 4096u + (unsigned)(li * 64) + (((unsigned)g ^ Ff((unsigned)(li >> 2))) << 4);

  auto issue = [&](int s) {
    const unsigned d = WB + (s & 3) * OP_STAGE;
    const char* ws = wo + (size_t)s * 32 * (DM * 2);
    glds(voffW0, ws, d);
    glds(voffW1, ws + 8 * (DM * 2), d + 1024);
    glds(voffW0, ws + 16 * (DM * 2), d + 2048);
    glds(voffW1, ws + 24 * (DM * 2), d + 3072);
    const char* os = oo + s * 64;
#pragma unroll
    for (int i = 0; i < 6; ++i) glds(voffO, os + (size_t)i * 16 * (NQ * 2), d + 4096 + i * 1024);
  };
  f32x4 acc[4][6];
#pragma unroll
  for (int T = 0; T < 4; ++T)
#pragma unroll
    for (int mt = 0; mt < 6; ++mt) acc[T][mt] = f32x4{0, 0, 0, 0};

  issue(0); issue(1); issue(2);
  auto step = [&](int s, auto VW, auto ISS) {
    waitv<decltype(VW)::value>();
    waitl0();
    if (decltype(ISS)::value) issue(s + 3);
    const unsigned d = WB + (s & 3) * OP_STAGE;
    bf16x8 af[4], bf[6];
#pragma unroll
    for (int T = 0; T < 4; ++T) { const unsigned a = d + (ta0 ^ (unsigned)(T << 5)); af[T] = lds_tr2(a, a + 512); }
#pragma unroll
    for (int mt = 0; mt < 6; ++mt) bf[mt] = lds_b128(d + ba0 + mt * 1024);
#pragma unroll
    for (int T = 0; T < 4; ++T)
#pragma unroll
      for (int mt = 0; mt < 6; ++mt) acc[T][mt] = mfma(af[T], bf[mt], acc[T][mt]);
  };
  using I0 = std::integral_constant<int, 0>;
  using I10 = std::integral_constant<int, 10>;
  using I20 = std::integral_constant<int, 20>;
  using T1 = std::integral_constant<bool, true>;
  using T0 = std::integral_constant<bool, false>;
  for (int s = 0; s < 29; ++s) step(s, I20{}, T1{});
  step(29, I20{}, T0{});
  step(30, I10{}, T0{});
  step(31, I0{}, T0{});
  waitl0();
#pragma unroll
  for (int T = 0; T < 4; ++T)
#pragma unroll
    for (int mt = 0; mt < 6; ++mt) *(LAS f32x4*)(uintptr_t)(WB + (T * 6 + mt) * 1024 + lane * 16) = acc[T][mt];
  bar();
  char* outp = P.out + ((size_t)m0 * (2 * DM) + p * DM + n0) * 2;
#pragma unroll
  for (int t = 0; t < 6; ++t) {
    const int id = w * 6 + t, T = id / 6, mt = id % 6;
    f32x4 s = {0, 0, 0, 0};
#pragma unroll
    for (int ww = 0; ww < 4; ++ww) s += *(const LAS f32x4*)(uintptr_t)(L0 + ww * OP_WAVE + id * 1024 + lane * 16);
    u32x2 v = {pk2(s[0], s[1]), pk2(s[2], s[3])};
    *(u32x2*)(outp + (size_t)(16 * mt + li) * (2 * DM * 2) + (16 * T + 4 * g) * 2) = v;
  }
}

extern "C" void kernel_launch(void* const* d_in, const int* in_sizes, int n_in, void* d_out, int out_size, void* d_ws,
                              size_t ws_size, hipStream_t stream) {
  AttnPtrs P;
  for (int p = 0; p < 2; ++p) {
    P.x[p] = (const char*)d_in[5 * p + 0];
    P.k[p] = (const char*)d_in[5 * p + 1];
    P.v[p] = (const char*)d_in[5 * p + 2];
    P.wq[p] = (const char*)d_in[5 * p + 3];
    P.wo[p] = (const char*)d_in[5 * p + 4];
  }
  P.o = (char*)d_ws;
  P.out = (char*)d_out;
  hipFuncSetAttribute((const void*)k_attn, hipFuncAttributeMaxDynamicSharedMemorySize, K1_LDS);
  hipFuncSetAttribute((const void*)k_oproj, hipFuncAttributeMaxDynamicSharedMemorySize, K2_LDS);
  k_attn<<<512, 256, K1_LDS, stream>>>(P);
  k_oproj<<<240, 256, K2_LDS, stream>>>(P);
}
```

```cpp
#include <hip/hip_runtime.h>
#include <hip/hip_bf16.h>
#include <stdint.h>

constexpr int NB = 8, SQ = 144, DM = 640, NH = 32, SK = 512, DH = 128;
constexpr int NQ = NH * DH;
constexpr int MROWS = NB * SQ;

typedef short s16x4 __attribute__((ext_vector_type(4)));
typedef short s16x8 __attribute__((ext_vector_type(8)));
typedef __bf16 bf16x8 __attribute__((ext_vector_type(8)));
typedef __bf16 bf16x2 __attribute__((ext_vector_type(2)));
typedef float f32x4 __attribute__((ext_vector_type(4)));
typedef float f32x2 __attribute__((ext_vector_type(2)));
typedef unsigned u32x4 __attribute__((ext_vector_type(4)));
typedef unsigned u32x2 __attribute__((ext_vector_type(2)));
#define LAS __attribute__((address_space(3)))
#define DI __device__ __forceinline__

template <int N> DI void waitv() { asm volatile("s_waitcnt vmcnt(%0)" ::"i"(N) : "memory"); }
DI void waitl0() { asm volatile("s_waitcnt lgkmcnt(0)" ::: "memory"); }
DI void bar() {
  asm volatile("s_waitcnt lgkmcnt(0)" ::: "memory");
  __builtin_amdgcn_s_barrier();
  asm volatile("" ::: "memory");
}
DI void glds_pair(unsigned lds, const void* sbase, unsigned voff) {
  asm volatile("s_mov_b32 m0, %0\n\ts_nop 0\n\t"
               "global_load_lds_dwordx4 %2, %1\n\t"
               "global_load_lds_dwordx4 %2, %1 offset:1024"
               ::"s"(lds), "s"(sbase), "v"(voff) : "memory", "m0");
}
DI void glds_quad(unsigned lds, const void* sbase, unsigned v0, unsigned v1, unsigned v2, unsigned v3) {
  asm volatile("s_mov_b32 m0, %0\n\ts_nop 0\n\t"
               "global_load_lds_dwordx4 %2, %1\n\t"
               "global_load_lds_dwordx4 %3, %1 offset:1024\n\t"
               "global_load_lds_dwordx4 %4, %1 offset:2048\n\t"
               "global_load_lds_dwordx4 %5, %1 offset:3072"
               ::"s"(lds), "s"(sbase), "v"(v0), "v"(v1), "v"(v2), "v"(v3) : "memory", "m0");
}
DI void glds_duo(unsigned lds, const void* sbase, unsigned v0, unsigned v1) {
  asm volatile("s_mov_b32 m0, %0\n\ts_nop 0\n\t"
               "global_load_lds_dwordx4 %2, %1\n\t"
               "global_load_lds_dwordx4 %3, %1 offset:1024"
               ::"s"(lds), "s"(sbase), "v"(v0), "v"(v1) : "memory", "m0");
}
DI void glds_qstage(unsigned lds, const void* sW, const void* sX, unsigned vXs, unsigned vW0, unsigned vW1, unsigned vX0, unsigned vX1) {
  asm volatile("s_mov_b32 m0, %0\n\ts_nop 0\n\t"
               "global_load_lds_dword %3, %2\n\t"
               "global_load_lds_dwordx4 %4, %1 offset:256\n\t"
               "global_load_lds_dwordx4 %5, %1 offset:1280\n\t"
               "global_load_lds_dwordx4 %6, %2 offset:2304\n\t"
               "global_load_lds_dwordx4 %7, %2 offset:3328"
               ::"s"(lds), "s"(sW), "s"(sX), "v"(vXs), "v"(vW0), "v"(vW1), "v"(vX0), "v"(vX1) : "memory", "m0");
}
DI void glds4(unsigned voff, const void* sbase, unsigned lds) {
  asm volatile("s_mov_b32 m0, %2\n\ts_nop 0\n\tglobal_load_lds_dword %0, %1" ::"v"(voff), "s"(sbase), "s"(lds) : "memory", "m0");
}
DI unsigned Fsw(unsigned j) { return (((j ^ (j >> 1)) & 1) << 1) | (j >> 1); }

#define CODE_PREFETCH(NAME, lane, ldsdst)                                                                      \
  do {                                                                                                         \
    unsigned long long pc_; unsigned sz_;                                                                      \
    asm volatile("s_getpc_b64 %0\n.Lpc_" NAME ":\n\ts_mov_b32 %1, .Lend_" NAME " - .Lpc_" NAME : "=s"(pc_), "=s"(sz_)); \
    for (unsigned b_ = 0; b_ < sz_; b_ += 4096u) {                                                             \
      unsigned o_ = b_ + (unsigned)(lane) * 64u; if (o_ + 4u > sz_) o_ = 0;                                    \
      glds4(o_, (const void*)pc_, (unsigned)(ldsdst));                                                         \
    }                                                                                                          \
  } while (0)
#define CODE_END(NAME) asm volatile(".Lend_" NAME ":")

DI bf16x8 lds_b128(unsigned a) { return __builtin_bit_cast(bf16x8, *(const LAS s16x8*)(uintptr_t)a); }
DI bf16x8 lds_tr2(unsigned a_lo, unsigned a_hi) {
  s16x4 lo = __builtin_amdgcn_ds_read_tr16_b64_v4i16((LAS s16x4*)(uintptr_t)a_lo);
  s16x4 hi = __builtin_amdgcn_ds_read_tr16_b64_v4i16((LAS s16x4*)(uintptr_t)a_hi);
  return __builtin_bit_cast(bf16x8, __builtin_shufflevector(lo, hi, 0, 1, 2, 3, 4, 5, 6, 7));
}
DI f32x4 mfma(bf16x8 a, bf16x8 b, f32x4 c) { return __builtin_amdgcn_mfma_f32_16x16x32_bf16(a, b, c, 0, 0, 0); }
DI unsigned pk2(float a, float b) {
  f32x2 v = {a, b};
  return __builtin_bit_cast(unsigned, __builtin_convertvector(v, bf16x2));
}
DI float xmax16_32(float v) {
  auto r = __builtin_amdgcn_permlane16_swap(__float_as_uint(v), __float_as_uint(v), false, false);
  v = fmaxf(__uint_as_float(r[0]), __uint_as_float(r[1]));
  auto s = __builtin_amdgcn_permlane32_swap(__float_as_uint(v), __float_as_uint(v), false, false);
  return fmaxf(__uint_as_float(s[0]), __uint_as_float(s[1]));
}
DI float max3(float a, float b, float c) { return fmaxf(fmaxf(a, b), c); }
DI float max8(const f32x4& a, const f32x4& b) { return max3(max3(a[0], a[1], a[2]), max3(a[3], b[0], b[1]), fmaxf(b[2], b[3])); }

#define STAMP(i) do {} while (0)
constexpr float SM_C = 0.088388347648318440f * 1.4426950408889634f;
constexpr float SM_THR = 12.0f;

constexpr int SLOT = 16384;
constexpr int K1_LDS = 5 * SLOT;

struct AttnPtrs {
  const char* x[2];
  const char* k[2];
  const char* v[2];
  const char* wq[2];
  const char* wo[2];
  char* o;
  char* out;
};

__global__ __launch_bounds__(256, 2) void k_attn(AttnPtrs P) {
  extern __shared__ __attribute__((aligned(16))) char smem[];
  const int tid = threadIdx.x, lane = tid & 63;
  const int w = __builtin_amdgcn_readfirstlane(tid >> 6);
  const int li = lane & 15, g = lane >> 4;
  const int blk = blockIdx.x;
  const int p = blk >> 8, idx = blk & 255, xcd = idx & 7, j = idx >> 3;
  const int h = 8 * (xcd & 3) + (j & 7), b = 4 * (xcd >> 2) + (j >> 3);

  const char* xb = P.x[p] + (size_t)b * SQ * DM * 2 - 4096;
  const char* wqh = P.wq[p] + h * 256 - 4096;
  const char* Kp = P.k[p] + (size_t)(b * NH + h) * SK * 256;
  const char* Vp = P.v[p] + (size_t)(b * NH + h) * SK * 256;

  const int q4 = (lane & 15) >> 2, p4 = lane & 3;
  const unsigned trsw = (unsigned)(8 * p4 + ((q4 | ((g & 1) << 2)) << 5));
  const unsigned tr0 = (unsigned)((8 * g + q4) * 256) + trsw;
  const int r4 = lane >> 4, c16 = lane & 15;
  const unsigned fV = (unsigned)(r4 | ((w & 1) << 2));
  const unsigned lcV = (((c16 >> 1) ^ fV) << 1) | (c16 & 1);
  const unsigned voffV = (unsigned)((8 * w + r4) * 256) + lcV * 16;
  const unsigned fK = (unsigned)((w << 2) | r4);
  const unsigned voffK = (unsigned)((8 * w + r4) * 256) + ((c16 ^ fK) * 16);
  const unsigned krow = (unsigned)(8 * (li >> 2) + (li & 3));

  auto issue_tile = [&](int tile, unsigned slot_off) {
    const char* src = ((tile & 1) ? Vp : Kp) + (size_t)(tile >> 1) * SLOT;
    const unsigned vo = (tile & 1) ? voffV : voffK;
    const unsigned d = slot_off + w * 2048;
    glds_pair(d, src, vo);
    glds_pair(d + 8192, src + 8192, vo);
  };

  STAMP(0);
  constexpr unsigned QRG = 4352, QST = 4 * QRG;
  f32x4 qa0[8], qa1[8], qaS[2];
#pragma unroll
  for (int i = 0; i < 8; ++i) { qa0[i] = f32x4{0, 0, 0, 0}; qa1[i] = f32x4{0, 0, 0, 0}; }
  qaS[0] = f32x4{0, 0, 0, 0}; qaS[1] = f32x4{0, 0, 0, 0};
  {
    const unsigned vW0 = 4096u - 256u + (unsigned)((8 * w + r4) * 8192) + lcV * 16;
    const unsigned vW1 = vW0 + 4 * 8192 - 1024;
    const unsigned vX0 = 4096u - 2304u + (unsigned)((32 * w + (lane >> 2)) * 1280) + (((unsigned)(lane & 3) ^ Fsw((unsigned)(lane >> 4))) * 16);
    const unsigned vX1 = vX0 + 16 * 1280 - 1024;
    const unsigned vXs = 4096u + (unsigned)((128 + 4 * w + (lane >> 4)) * 1280) + ((((unsigned)(lane & 15) >> 2) ^ Fsw((unsigned)w)) * 16) + (unsigned)(lane & 3) * 4;
    unsigned ta[8];
#pragma unroll
    for (int i = 0; i < 8; ++i) {
      ta[i] = (unsigned)(g * QRG + 256 + q4 * 256) + (trsw ^ (unsigned)(((i + 2 * w) & 7) << 5));
      asm volatile("" : "+v"(ta[i]));
    }
    unsigned xa = (unsigned)(w * QRG + 2304 + li * 64) + (((unsigned)g ^ Fsw((unsigned)(li >> 2))) << 4);
    unsigned xs_ = (unsigned)((li >> 2) * QRG + (li & 3) * 64) + (((unsigned)g ^ Fsw((unsigned)(li >> 2))) << 4);
    asm volatile("" : "+v"(xa), "+v"(xs_));
    auto issue_q = [&](int s, int u) {
      glds_qstage(u * QST + w * QRG, wqh + (size_t)s * 32 * 8192, xb + s * 64, vXs, vW0, vW1, vX0, vX1);
    };
    if (w == 0) CODE_PREFETCH("k1", lane, 68 * 1024);
#pragma unroll
    for (int e = 0; e < 2; ++e) {
      const int piece = 2 * w + e;
      if (piece < 5) {
        const unsigned row = (unsigned)(20 * j + 4 * piece + (lane >> 4));
        glds4(row * 8192u + (unsigned)((xcd & 3) * 2048) + (unsigned)(lane & 15) * 128u, P.wq[p], 68 * 1024 + 256 * w);
      } else {
        unsigned line = (unsigned)(180 * j + 64 * (piece - 5) + lane); if (line >= 5760u) line = 5759u;
        glds4(line * 128u, P.x[p] + (size_t)(4 * (xcd >> 2)) * SQ * DM * 2, 68 * 1024 + 256 * w);
      }
    }
#pragma unroll
    for (int u = 0; u < 4; ++u) issue_q(u, u);
    bf16x8 A[2][8], Bx[2][3];
    waitv<15>();
    bar();
    STAMP(5);
#pragma unroll
    for (int i = 0; i < 8; ++i) A[0][i] = lds_tr2(ta[i], ta[i] + 1024);
    Bx[0][0] = lds_b128(xa); Bx[0][1] = lds_b128(xa + 1024); Bx[0][2] = lds_b128(xs_);
#pragma nounroll
    for (int it = 0; it < 5; ++it) {
      const bool last = (it == 4);
      STAMP(8 + it);
#pragma unroll
      for (int u = 0; u < 4; ++u) {
        const int s = 4 * it + u;
        const int n = (u + 1) & 3;
        if (!(last && u == 3)) {
          if (!last || u == 0) waitv<10>();
          else if (u == 1) waitv<9>();
          else waitv<8>();
          bar();
          if (!last) issue_q(s + 4, u);
          else issue_tile(u, u * SLOT);
          Bx[(u + 1) & 1][0] = lds_b128(xa + n * QST); Bx[(u + 1) & 1][1] = lds_b128(xa + n * QST + 1024); Bx[(u + 1) & 1][2] = lds_b128(xs_ + n * QST);
        }
#pragma unroll
        for (int i = 0; i < 8; ++i) {
          if (!(last && u == 3)) { const unsigned a = ta[i] + n * QST; A[(u + 1) & 1][i] = lds_tr2(a, a + 1024); }
          qa0[i] = mfma(A[u & 1][i], Bx[u & 1][0], qa0[i]);
          qa1[i] = mfma(A[u & 1][i], Bx[u & 1][1], qa1[i]);
          if (i < 2) qaS[i] = mfma(A[u & 1][i], Bx[u & 1][2], qaS[i]);
          __builtin_amdgcn_sched_barrier(0);
        }
      }
    }
  }
  STAMP(1);
  bf16x8 Q0[4], Q1[4], QS[4];
  {
    bar();
    const unsigned own = 3 * SLOT + w * 4096, shr = 4 * SLOT;
    auto waddr = [&](unsigned base, int T) { return base + li * 256 + ((((2 * T + (g >> 1)) ^ li) & 15) << 4) + 8 * (g & 1); };
    auto raddr = [&](unsigned base, int ds) { return base + li * 256 + ((((4 * ds + g) ^ li) & 15) << 4); };
    auto pkq = [&](const f32x4& a) { u32x2 v = {pk2(a[0] * SM_C, a[1] * SM_C), pk2(a[2] * SM_C, a[3] * SM_C)}; return v; };
#pragma unroll
    for (int t = 0; t < 2; ++t) *(LAS u32x2*)(uintptr_t)waddr(shr, 2 * w + t) = pkq(qaS[t]);
#pragma unroll
    for (int i = 0; i < 8; ++i) *(LAS u32x2*)(uintptr_t)waddr(own, (i + 2 * w) & 7) = pkq(qa0[i]);
    waitl0();
#pragma unroll
    for (int ds = 0; ds < 4; ++ds) Q0[ds] = lds_b128(raddr(own, ds));
    waitl0();
#pragma unroll
    for (int i = 0; i < 8; ++i) *(LAS u32x2*)(uintptr_t)waddr(own, (i + 2 * w) & 7) = pkq(qa1[i]);
    waitl0();
#pragma unroll
    for (int ds = 0; ds < 4; ++ds) Q1[ds] = lds_b128(raddr(own, ds));
    bar();
#pragma unroll
    for (int ds = 0; ds < 4; ++ds) QS[ds] = lds_b128(raddr(shr, ds));
    bar();
    issue_tile(3, 3 * SLOT);
  }

  STAMP(2);
  f32x4 O0[8], O1[8], OS[8];
#pragma unroll
  for (int i = 0; i < 8; ++i) { O0[i] = f32x4{0, 0, 0, 0}; O1[i] = f32x4{0, 0, 0, 0}; OS[i] = f32x4{0, 0, 0, 0}; }
  f32x4 L0a = {0, 0, 0, 0}, L1a = {0, 0, 0, 0}, LSa = {0, 0, 0, 0};
  f32x4 n0 = {0, 0, 0, 0}, n1 = {0, 0, 0, 0}, nS = {0, 0, 0, 0};
  unsigned ka[4];
#pragma unroll
  for (int ds = 0; ds < 4; ++ds) ka[ds] = krow * 256 + ((((unsigned)(4 * ds + g) ^ (unsigned)li) & 15) << 4);
  const s16x8 ones_s = {0x3F80, 0x3F80, 0x3F80, 0x3F80, 0x3F80, 0x3F80, 0x3F80, 0x3F80};
  const bf16x8 ones = __builtin_bit_cast(bf16x8, ones_s);

  {
    unsigned sK = 0, sV = SLOT, sNK = 4 * SLOT;
    const unsigned cS = (unsigned)(w & 1) * 8192;
#pragma nounroll
    for (int it = 0; it < 8; ++it) {
      if (it < 7) waitv<12>(); else waitv<4>();
      bar();
      STAMP(16 + it);
      if (it < 6) issue_tile(2 * it + 4, sNK);
      f32x4 S0[4], S1[4];
      {
        unsigned kb[4];
#pragma unroll
        for (int ds = 0; ds < 4; ++ds) kb[ds] = ka[ds] + sK;
        bf16x8 F[2][4];
#pragma unroll
        for (int ds = 0; ds < 4; ++ds) F[0][ds] = lds_b128(kb[ds]);
#pragma unroll
        for (int kt = 0; kt < 4; ++kt) {
          if (kt < 3) {
#pragma unroll
            for (int ds = 0; ds < 4; ++ds) F[(kt + 1) & 1][ds] = lds_b128(kb[ds] + (32 * ((kt + 1) >> 1) + 4 * ((kt + 1) & 1)) * 256);
          }
          S0[kt] = mfma(F[kt & 1][0], Q0[0], n0);
          S1[kt] = mfma(F[kt & 1][0], Q1[0], n1);
#pragma unroll
          for (int ds = 1; ds < 4; ++ds) {
            S0[kt] = mfma(F[kt & 1][ds], Q0[ds], S0[kt]);
            S1[kt] = mfma(F[kt & 1][ds], Q1[ds], S1[kt]);
          }
          __builtin_amdgcn_sched_barrier(0);
        }
      }
      const bool shared_on = ((it & 1) == (w >> 1));
      const bool firstS = (it < 2);
      bf16x8 PS;
      if (shared_on) {
        f32x4 SS[2];
        {
          bf16x8 F[2][4];
#pragma unroll
          for (int t = 0; t < 2; ++t)
#pragma unroll
            for (int ds = 0; ds < 4; ++ds) F[t][ds] = lds_b128(ka[ds] + sK + cS + t * 1024);
          SS[0] = mfma(F[0][0], QS[0], nS);
          SS[1] = mfma(F[1][0], QS[0], nS);
#pragma unroll
          for (int ds = 1; ds < 4; ++ds) {
            SS[0] = mfma(F[0][ds], QS[ds], SS[0]);
            SS[1] = mfma(F[1][ds], QS[ds], SS[1]);
          }
        }
        float mx = xmax16_32(max8(SS[0], SS[1]));
        if (firstS || __any(mx > SM_THR)) {
          const float d = firstS ? mx : fmaxf(mx, 0.f);
          nS -= d; SS[0] -= d; SS[1] -= d;
          if (!firstS) {
            const float alpha = __builtin_amdgcn_exp2f(-d);
#pragma unroll
            for (int dt = 0; dt < 8; ++dt) OS[dt] *= alpha;
            LSa *= alpha;
          }
        }
#pragma unroll
        for (int t = 0; t < 2; ++t)
#pragma unroll
          for (int r = 0; r < 4; ++r) SS[t][r] = __builtin_amdgcn_exp2f(SS[t][r]);
        u32x4 u = {pk2(SS[0][0], SS[0][1]), pk2(SS[0][2], SS[0][3]), pk2(SS[1][0], SS[1][1]), pk2(SS[1][2], SS[1][3])};
        PS = __builtin_bit_cast(bf16x8, u);
      }
      bf16x8 P0[2], P1[2];
      {
        float mx0 = xmax16_32(fmaxf(max8(S0[0], S0[1]), max8(S0[2], S0[3])));
        float mx1 = xmax16_32(fmaxf(max8(S1[0], S1[1]), max8(S1[2], S1[3])));
        if (it == 0 || __any(fmaxf(mx0, mx1) > SM_THR)) {
          const float d0 = (it == 0) ? mx0 : fmaxf(mx0, 0.f), d1 = (it == 0) ? mx1 : fmaxf(mx1, 0.f);
          n0 -= d0; n1 -= d1;
#pragma unroll
          for (int kt = 0; kt < 4; ++kt) { S0[kt] -= d0; S1[kt] -= d1; }
          if (it != 0) {
            const float a0 = __builtin_amdgcn_exp2f(-d0), a1 = __builtin_amdgcn_exp2f(-d1);
#pragma unroll
            for (int dt = 0; dt < 8; ++dt) { O0[dt] *= a0; O1[dt] *= a1; }
            L0a *= a0; L1a *= a1;
          }
        }
#pragma unroll
        for (int kt = 0; kt < 4; ++kt)
#pragma unroll
          for (int r = 0; r < 4; ++r) { S0[kt][r] = __builtin_amdgcn_exp2f(S0[kt][r]); S1[kt][r] = __builtin_amdgcn_exp2f(S1[kt][r]); }
#pragma unroll
        for (int c = 0; c < 2; ++c) {
          u32x4 u = {pk2(S0[2 * c][0], S0[2 * c][1]), pk2(S0[2 * c][2], S0[2 * c][3]), pk2(S0[2 * c + 1][0], S0[2 * c + 1][1]), pk2(S0[2 * c + 1][2], S0[2 * c + 1][3])};
          P0[c] = __builtin_bit_cast(bf16x8, u);
          u32x4 v = {pk2(S1[2 * c][0], S1[2 * c][1]), pk2(S1[2 * c][2], S1[2 * c][3]), pk2(S1[2 * c + 1][0], S1[2 * c + 1][1]), pk2(S1[2 * c + 1][2], S1[2 * c + 1][3])};
          P1[c] = __builtin_bit_cast(bf16x8, v);
        }
      }
      L0a = mfma(ones, P0[0], L0a); L1a = mfma(ones, P1[0], L1a);
      L0a = mfma(ones, P0[1], L0a); L1a = mfma(ones, P1[1], L1a);
      if (it < 6) waitv<12>(); else if (it == 6) waitv<8>(); else waitv<0>();
      bar();
      if (it < 6) issue_tile(2 * it + 5, sK);
      const unsigned vb = tr0 + sV;
      {
        bf16x8 G[3][2];
#pragma unroll
        for (int dt = 0; dt < 2; ++dt)
#pragma unroll
          for (int c = 0; c < 2; ++c) { const unsigned a = (vb ^ (unsigned)(dt << 5)) + c * 8192; G[dt][c] = lds_tr2(a, a + 1024); }
#pragma unroll
        for (int dt = 0; dt < 8; ++dt) {
          if (dt + 2 < 8) {
#pragma unroll
            for (int c = 0; c < 2; ++c) { const unsigned a = (vb ^ (unsigned)((dt + 2) << 5)) + c * 8192; G[(dt + 2) % 3][c] = lds_tr2(a, a + 1024); }
          }
          O0[dt] = mfma(G[dt % 3][0], P0[0], O0[dt]);
          O1[dt] = mfma(G[dt % 3][0], P1[0], O1[dt]);
          O0[dt] = mfma(G[dt % 3][1], P0[1], O0[dt]);
          O1[dt] = mfma(G[dt % 3][1], P1[1], O1[dt]);
          __builtin_amdgcn_sched_barrier(0);
        }
      }
      if (shared_on) {
        LSa = mfma(ones, PS, LSa);
#pragma unroll
        for (int hh = 0; hh < 2; ++hh) {
          bf16x8 G[4];
#pragma unroll
          for (int d4 = 0; d4 < 4; ++d4) { const unsigned a = (vb ^ (unsigned)((4 * hh + d4) << 5)) + cS; G[d4] = lds_tr2(a, a + 1024); }
#pragma unroll
          for (int d4 = 0; d4 < 4; ++d4) OS[4 * hh + d4] = mfma(G[d4], PS, OS[4 * hh + d4]);
        }
      }
      sNK = sV;
      sK += 2 * SLOT; if (sK >= 5 * SLOT) sK -= 5 * SLOT;
      sV += 2 * SLOT; if (sV >= 5 * SLOT) sV -= 5 * SLOT;
    }
  }

  STAMP(3);
  char* ob = P.o + (size_t)((p * NB + b) * NH + h) * (9 * 4096) + lane * 16;
  {
    const unsigned so = SLOT + w * 8192 + lane * 16;
#pragma unroll
    for (int dt = 0; dt < 8; ++dt) *(LAS f32x4*)(uintptr_t)(so + dt * 1024) = OS[dt];
    if (g == 0) {
      f32x2 ml = {-nS[0], LSa[0]};
      *(LAS f32x2*)(uintptr_t)(3 * SLOT + (w * 16 + li) * 8) = ml;
    }
  }
  {
    const float i0 = 1.0f / L0a[0], i1 = 1.0f / L1a[0];
    char* r0 = ob + (size_t)(2 * w) * 4096;
#pragma unroll
    for (int ds = 0; ds < 4; ++ds) {
      u32x4 v0 = {pk2(O0[2 * ds][0] * i0, O0[2 * ds][1] * i0), pk2(O0[2 * ds][2] * i0, O0[2 * ds][3] * i0),
                  pk2(O0[2 * ds + 1][0] * i0, O0[2 * ds + 1][1] * i0), pk2(O0[2 * ds + 1][2] * i0, O0[2 * ds + 1][3] * i0)};
      u32x4 v1 = {pk2(O1[2 * ds][0] * i1, O1[2 * ds][1] * i1), pk2(O1[2 * ds][2] * i1, O1[2 * ds][3] * i1),
                  pk2(O1[2 * ds + 1][0] * i1, O1[2 * ds + 1][1] * i1), pk2(O1[2 * ds + 1][2] * i1, O1[2 * ds + 1][3] * i1)};
      *(u32x4*)(r0 + ds * 1024) = v0;
      *(u32x4*)(r0 + 4096 + ds * 1024) = v1;
    }
  }
  bar();
  {
    float mm[4], ll[4];
#pragma unroll
    for (int ww = 0; ww < 4; ++ww) {
      f32x2 ml = *(const LAS f32x2*)(uintptr_t)(3 * SLOT + (ww * 16 + li) * 8);
      mm[ww] = ml[0]; ll[ww] = ml[1];
    }
    const float M = fmaxf(fmaxf(mm[0], mm[1]), fmaxf(mm[2], mm[3]));
    float sc[4], L = 0.f;
#pragma unroll
    for (int ww = 0; ww < 4; ++ww) { sc[ww] = __builtin_amdgcn_exp2f(mm[ww] - M); L += sc[ww] * ll[ww]; }
    const float inv = 1.0f / L;
    f32x4 acc[2] = {f32x4{0, 0, 0, 0}, f32x4{0, 0, 0, 0}};
#pragma unroll
    for (int t = 0; t < 2; ++t)
#pragma unroll
      for (int ww = 0; ww < 4; ++ww) {
        const f32x4 v = *(const LAS f32x4*)(uintptr_t)(SLOT + ww * 8192 + (2 * w + t) * 1024 + lane * 16);
        acc[t] += v * sc[ww];
      }
    u32x4 v = {pk2(acc[0][0] * inv, acc[0][1] * inv), pk2(acc[0][2] * inv, acc[0][3] * inv),
               pk2(acc[1][0] * inv, acc[1][1] * inv), pk2(acc[1][2] * inv, acc[1][3] * inv)};
    *(u32x4*)(ob + (size_t)8 * 4096 + w * 1024) = v;
  }
  STAMP(4);
  CODE_END("k1");
}

constexpr int OP_STAGE = 4096 + 6144;
constexpr int OP_WAVE = 4 * OP_STAGE;
constexpr int K2_LDS = 4 * OP_WAVE;

__global__ __launch_bounds__(256, 1) void k_oproj(AttnPtrs P) {
  extern __shared__ __attribute__((aligned(16))) char smem[];
  const int tid = threadIdx.x, lane = tid & 63;
  const int w = __builtin_amdgcn_readfirstlane(tid >> 6);
  const int li = lane & 15, g = lane >> 4;
  const int blk = blockIdx.x;
  const int xcd = blk & 7, jj = blk >> 3;
  const int p = xcd >> 2, mt0 = 3 * (xcd & 3) + jj / 10, nt0 = jj % 10;
  const int m0 = mt0 * 96, n0 = nt0 * 64;
  const char* wo = P.wo[p] + (size_t)(w * 1024) * (DM * 2) + n0 * 2 - 4096;
  const unsigned WB = (unsigned)(w * OP_WAVE);
  const char* ob = P.o + (size_t)(p * NB * NH + 8 * w) * 9 * 4096 - 8192;
  unsigned voffB[6];
#pragma unroll
  for (int i = 0; i < 6; ++i) {
    const int t = mt0 * 6 + i, b = t / 9, qt = t % 9;
    voffB[i] = 8192u - (unsigned)((i & 3) * 1024) + (unsigned)((b * NH * 9 + qt) * 4096 + lane * 16);
  }
  const int r8 = lane >> 3, c8 = lane & 7;
  unsigned voffW[4];
#pragma unroll
  for (int i = 0; i < 4; ++i)
    voffW[i] = 4096u - (unsigned)(i * 1024) + (unsigned)((8 * i + r8) * (DM * 2)) + (((((unsigned)(c8 >> 1)) ^ (unsigned)((lane >> 4) & 3)) << 1 | (c8 & 1)) * 16);
  const int q4 = li >> 2, p4 = lane & 3;
  const unsigned rr = (unsigned)(4 * g + q4);
  unsigned ta[4];
#pragma unroll
  for (int T = 0; T < 4; ++T) { ta[T] = WB + rr * 128 + 8 * p4 + ((((unsigned)T) ^ ((rr >> 1) & 3)) << 5); asm volatile("" : "+v"(ta[T])); }
  unsigned ba = WB + 4096 + lane * 16;
  asm volatile("" : "+v"(ba));

  auto issue = [&](int hh, int u) {
    const unsigned d = WB + u * OP_STAGE;
    glds_quad(d, wo + (size_t)(hh * 4 + u) * 32 * (DM * 2), voffW[0], voffW[1], voffW[2], voffW[3]);
    const char* os = ob + (size_t)hh * (9 * 4096) + u * 1024;
    glds_quad(d + 4096, os, voffB[0], voffB[1], voffB[2], voffB[3]);
    glds_duo(d + 8192, os, voffB[4], voffB[5]);
  };
  f32x4 acc[4][6];
#pragma unroll
  for (int T = 0; T < 4; ++T)
#pragma unroll
    for (int mt = 0; mt < 6; ++mt) acc[T][mt] = f32x4{0, 0, 0, 0};

  issue(0, 0); issue(0, 1); issue(0, 2); issue(0, 3);
  bf16x8 af[2][4], bf[2][6];
  waitv<30>();
#pragma unroll
  for (int T = 0; T < 4; ++T) af[0][T] = lds_tr2(ta[T], ta[T] + 2048);
#pragma unroll
  for (int mt = 0; mt < 6; ++mt) bf[0][mt] = lds_b128(ba + mt * 1024);
  waitl0();
  issue(1, 0);
#pragma nounroll
  for (int hh = 0; hh < 8; ++hh) {
#pragma unroll
    for (int u = 0; u < 4; ++u) {
      const int n = (u + 1) & 3;
      const bool lastu = (hh == 7);
      if (!(lastu && u == 3)) {
        if (!lastu) waitv<30>();
        else if (u == 0) waitv<20>();
        else if (u == 1) waitv<10>();
        else waitv<0>();
#pragma unroll
        for (int T = 0; T < 4; ++T) { const unsigned a = ta[T] + n * OP_STAGE; af[(u + 1) & 1][T] = lds_tr2(a, a + 2048); }
#pragma unroll
        for (int mt = 0; mt < 6; ++mt) bf[(u + 1) & 1][mt] = lds_b128(ba + n * OP_STAGE + mt * 1024);
      }
#pragma unroll
      for (int T = 0; T < 4; ++T)
#pragma unroll
        for (int mt = 0; mt < 6; ++mt) acc[T][mt] = mfma(af[u & 1][T], bf[u & 1][mt], acc[T][mt]);
      __builtin_amdgcn_sched_barrier(0);
      waitl0();
      const int s5 = 4 * hh + u + 5;
      if (s5 < 32) issue(s5 >> 2, n);
    }
  }
#pragma unroll
  for (int T = 0; T < 4; ++T)
#pragma unroll
    for (int mt = 0; mt < 6; ++mt) *(LAS f32x4*)(uintptr_t)(WB + (T * 6 + mt) * 1024 + lane * 16) = acc[T][mt];
  bar();
  char* outp = P.out + ((size_t)m0 * (2 * DM) + p * DM + n0) * 2;
#pragma unroll
  for (int t = 0; t < 6; ++t) {
    const int id = w * 6 + t, T = w, mt = t;
    f32x4 s = {0, 0, 0, 0};
#pragma unroll
    for (int ww = 0; ww < 4; ++ww) s += *(const LAS f32x4*)(uintptr_t)(ww * OP_WAVE + id * 1024 + lane * 16);
    u32x2 v = {pk2(s[0], s[1]), pk2(s[2], s[3])};
    *(u32x2*)(outp + (size_t)(16 * mt + li) * (2 * DM * 2) + (16 * T + 4 * g) * 2) = v;
  }
}

extern "C" void kernel_launch(void* const* d_in, const int* in_sizes, int n_in, void* d_out, int out_size, void* d_ws,
                              size_t ws_size, hipStream_t stream) {
  AttnPtrs P;
  for (int p = 0; p < 2; ++p) {
    P.x[p] = (const char*)d_in[5 * p + 0];
    P.k[p] = (const char*)d_in[5 * p + 1];
    P.v[p] = (const char*)d_in[5 * p + 2];
    P.wq[p] = (const char*)d_in[5 * p + 3];
    P.wo[p] = (const char*)d_in[5 * p + 4];
  }
  P.o = (char*)d_ws;
  P.out = (char*)d_out;
  (void)hipFuncSetAttribute((const void*)k_attn, hipFuncAttributeMaxDynamicSharedMemorySize, K1_LDS);
  (void)hipFuncSetAttribute((const void*)k_oproj, hipFuncAttributeMaxDynamicSharedMemorySize, K2_LDS);
  k_attn<<<512, 256, K1_LDS, stream>>>(P);
  k_oproj<<<240, 256, K2_LDS, stream>>>(P);
}
```

```cpp
#include <hip/hip_runtime.h>
#include <hip/hip_bf16.h>
#include <stdint.h>

constexpr int NB = 8, SQ = 144, DM = 640, NH = 32, SK = 512, DH = 128;
constexpr int NQ = NH * DH;
constexpr int MROWS = NB * SQ;

typedef short s16x4 __attribute__((ext_vector_type(4)));
typedef short s16x8 __attribute__((ext_vector_type(8)));
typedef __bf16 bf16x8 __attribute__((ext_vector_type(8)));
typedef __bf16 bf16x2 __attribute__((ext_vector_type(2)));
typedef float f32x4 __attribute__((ext_vector_type(4)));
typedef float f32x2 __attribute__((ext_vector_type(2)));
typedef unsigned u32x4 __attribute__((ext_vector_type(4)));
typedef unsigned u32x2 __attribute__((ext_vector_type(2)));
#define LAS __attribute__((address_space(3)))
#define DI __device__ __forceinline__

template <int N> DI void waitv() { asm volatile("s_waitcnt vmcnt(%0)" ::"i"(N) : "memory"); }
DI void waitl0() { asm volatile("s_waitcnt lgkmcnt(0)" ::: "memory"); }
DI void bar() {
  asm volatile("s_waitcnt lgkmcnt(0)" ::: "memory");
  __builtin_amdgcn_s_barrier();
  asm volatile("" ::: "memory");
}
DI void glds_pair_nt(unsigned lds, const void* sbase, unsigned voff) {
  asm volatile("s_mov_b32 m0, %0\n\ts_nop 0\n\t"
               "global_load_lds_dwordx4 %2, %1 nt\n\t"
               "global_load_lds_dwordx4 %2, %1 offset:1024 nt"
               ::"s"(lds), "s"(sbase), "v"(voff) : "memory", "m0");
}
DI void glds_quad(unsigned lds, const void* sbase, unsigned v0, unsigned v1, unsigned v2, unsigned v3) {
  asm volatile("s_mov_b32 m0, %0\n\ts_nop 0\n\t"
               "global_load_lds_dwordx4 %2, %1\n\t"
               "global_load_lds_dwordx4 %3, %1 offset:1024\n\t"
               "global_load_lds_dwordx4 %4, %1 offset:2048\n\t"
               "global_load_lds_dwordx4 %5, %1 offset:3072"
               ::"s"(lds), "s"(sbase), "v"(v0), "v"(v1), "v"(v2), "v"(v3) : "memory", "m0");
}
DI void glds_duo(unsigned lds, const void* sbase, unsigned v0, unsigned v1) {
  asm volatile("s_mov_b32 m0, %0\n\ts_nop 0\n\t"
               "global_load_lds_dwordx4 %2, %1\n\t"
               "global_load_lds_dwordx4 %3, %1 offset:1024"
               ::"s"(lds), "s"(sbase), "v"(v0), "v"(v1) : "memory", "m0");
}
DI void glds4(unsigned voff, const void* sbase, unsigned lds) {
  asm volatile("s_mov_b32 m0, %2\n\ts_nop 0\n\tglobal_load_lds_dword %0, %1" ::"v"(voff), "s"(sbase), "s"(lds) : "memory", "m0");
}
template <int OFF> DI void glds1(unsigned lds, const void* sbase, unsigned voff) {
  asm volatile("s_mov_b32 m0, %0\n\ts_nop 0\n\tglobal_load_lds_dwordx4 %2, %1 offset:%3" ::"s"(lds), "s"(sbase), "v"(voff), "i"(OFF) : "memory", "m0");
}
template <int OFF> DI void glds1nt(unsigned lds, const void* sbase, unsigned voff) {
  asm volatile("s_mov_b32 m0, %0\n\ts_nop 0\n\tglobal_load_lds_dwordx4 %2, %1 offset:%3 nt" ::"s"(lds), "s"(sbase), "v"(voff), "i"(OFF) : "memory", "m0");
}
DI unsigned Fsw(unsigned j) { return (((j ^ (j >> 1)) & 1) << 1) | (j >> 1); }

#define CODE_PREFETCH(NAME, lane, ldsdst)                                                                      \
  do {                                                                                                         \
    unsigned long long pc_; unsigned sz_;                                                                      \
    asm volatile("s_getpc_b64 %0\n.Lpc_" NAME ":\n\ts_mov_b32 %1, .Lend_" NAME " - .Lpc_" NAME : "=s"(pc_), "=s"(sz_)); \
    for (unsigned b_ = 0; b_ < sz_; b_ += 4096u) {                                                             \
      unsigned o_ = b_ + (unsigned)(lane) * 64u; if (o_ + 4u > sz_) o_ = 0;                                    \
      glds4(o_, (const void*)pc_, (unsigned)(ldsdst));                                                         \
    }                                                                                                          \
  } while (0)
#define CODE_END(NAME) asm volatile(".Lend_" NAME ":")

DI bf16x8 lds_b128(unsigned a) { return __builtin_bit_cast(bf16x8, *(const LAS s16x8*)(uintptr_t)a); }
DI bf16x8 lds_tr2(unsigned a_lo, unsigned a_hi) {
  s16x4 lo = __builtin_amdgcn_ds_read_tr16_b64_v4i16((LAS s16x4*)(uintptr_t)a_lo);
  s16x4 hi = __builtin_amdgcn_ds_read_tr16_b64_v4i16((LAS s16x4*)(uintptr_t)a_hi);
  return __builtin_bit_cast(bf16x8, __builtin_shufflevector(lo, hi, 0, 1, 2, 3, 4, 5, 6, 7));
}
DI f32x4 mfma(bf16x8 a, bf16x8 b, f32x4 c) { return __builtin_amdgcn_mfma_f32_16x16x32_bf16(a, b, c, 0, 0, 0); }
DI unsigned pk2(float a, float b) {
  f32x2 v = {a, b};
  return __builtin_bit_cast(unsigned, __builtin_convertvector(v, bf16x2));
}
DI float xmax16_32(float v) {
  auto r = __builtin_amdgcn_permlane16_swap(__float_as_uint(v), __float_as_uint(v), false, false);
  v = fmaxf(__uint_as_float(r[0]), __uint_as_float(r[1]));
  auto s = __builtin_amdgcn_permlane32_swap(__float_as_uint(v), __float_as_uint(v), false, false);
  return fmaxf(__uint_as_float(s[0]), __uint_as_float(s[1]));
}
DI float max3(float a, float b, float c) { float r; asm("v_max3_f32 %0, %1, %2, %3" : "=v"(r) : "v"(a), "v"(b), "v"(c)); return r; }
DI float max8(const f32x4& a, const f32x4& b) { return max3(max3(a[0], a[1], a[2]), max3(a[3], b[0], b[1]), max3(b[2], b[3], b[3])); }

constexpr float SM_C = 0.088388347648318440f * 1.4426950408889634f;
constexpr float SM_THR = 12.0f;

constexpr int SLOT = 16384;
constexpr int K1_LDS = 5 * SLOT;

struct AttnPtrs {
  const char* x[2];
  const char* k[2];
  const char* v[2];
  const char* wq[2];
  const char* wo[2];
  char* o;
  char* out;
};

__global__ __launch_bounds__(256, 2) void k_attn(AttnPtrs P) {
  extern __shared__ __attribute__((aligned(16))) char smem[];
  const int tid = threadIdx.x, lane = tid & 63;
  const int w = __builtin_amdgcn_readfirstlane(tid >> 6);
  const int li = lane & 15, g = lane >> 4;
  const int blk = blockIdx.x;
  const int p = blk >> 8, idx = blk & 255, xcd = idx & 7, j = idx >> 3;
  const int h = 8 * (xcd & 3) + (j & 7), b = 4 * (xcd >> 2) + (j >> 3);

  const char* Kp = P.k[p] + (size_t)(b * NH + h) * SK * 256;
  const char* Vp = P.v[p] + (size_t)(b * NH + h) * SK * 256;

  const int q4 = (lane & 15) >> 2, p4 = lane & 3;
  const unsigned trsw = (unsigned)(8 * p4 + ((q4 | ((g & 1) << 2)) << 5));
  const unsigned tr0 = (unsigned)((8 * g + q4) * 256) + trsw;
  const int r4 = lane >> 4, c16 = lane & 15;
  const unsigned fV = (unsigned)(r4 | ((w & 1) << 2));
  const unsigned lcV = (((c16 >> 1) ^ fV) << 1) | (c16 & 1);
  const unsigned voffV = (unsigned)((8 * w + r4) * 256) + lcV * 16;
  const unsigned fK = (unsigned)((w << 2) | r4);
  const unsigned voffK = (unsigned)((8 * w + r4) * 256) + ((c16 ^ fK) * 16);
  const unsigned krow = (unsigned)(8 * (li >> 2) + (li & 3));

  auto issue_tile = [&](int tile, unsigned slot_off) {
    const char* src = ((tile & 1) ? Vp : Kp) + (size_t)(tile >> 1) * SLOT;
    const unsigned vo = (tile & 1) ? voffV : voffK;
    const unsigned d = slot_off + w * 2048;
    glds_pair_nt(d, src, vo);
    glds_pair_nt(d + 8192, src + 8192, vo);
  };

  auto issue_piece = [&](int tile, unsigned slot_off, int piece) {
    const char* src = ((tile & 1) ? Vp : Kp) + (size_t)(tile >> 1) * SLOT + (piece >> 1) * 8192;
    const unsigned vo = (tile & 1) ? voffV : voffK;
    const unsigned d = slot_off + w * 2048 + (piece >> 1) * 8192;
    if (piece & 1) glds1nt<1024>(d, src, vo); else glds1nt<0>(d, src, vo);
  };

  constexpr unsigned XB = 32768, XSZ = 18432;
  f32x4 qa0[8], qa1[8], qaS[2];
#pragma unroll
  for (int i = 0; i < 8; ++i) { qa0[i] = f32x4{0, 0, 0, 0}; qa1[i] = f32x4{0, 0, 0, 0}; }
  qaS[0] = f32x4{0, 0, 0, 0}; qaS[1] = f32x4{0, 0, 0, 0};
  {
    const unsigned vWa = (unsigned)(r4 * 8192) + (((((unsigned)c16 >> 1) ^ (unsigned)r4) << 1 | (c16 & 1)) * 16);
    const unsigned vWb = (unsigned)(r4 * 8192) + (((((unsigned)c16 >> 1) ^ (unsigned)(r4 | 4)) << 1 | (c16 & 1)) * 16);
    const unsigned vXa = (unsigned)((lane >> 3) * 1280) + ((((unsigned)lane & 7) ^ (unsigned)(lane >> 4)) * 16);
    const unsigned vXb = (unsigned)((lane >> 3) * 1280) + ((((unsigned)lane & 7) ^ (unsigned)(4 | (lane >> 4))) * 16);
    const char* wq0 = P.wq[p] + h * 256;
    const char* x0 = P.x[p] + (size_t)b * SQ * DM * 2;
    unsigned ta[8];
#pragma unroll
    for (int i = 0; i < 8; ++i) { ta[i] = tr0 ^ (unsigned)(((i + 2 * w) & 7) << 5); asm volatile("" : "+v"(ta[i])); }
    unsigned xa[2], xs_[2];
#pragma unroll
    for (int hf = 0; hf < 2; ++hf) {
      xa[hf] = XB + (unsigned)((32 * w + li) * 128) + ((((unsigned)(4 * hf + g)) ^ (unsigned)((li >> 1) & 7)) << 4);
      xs_[hf] = XB + (unsigned)((128 + li) * 128) + ((((unsigned)(4 * hf + g)) ^ (unsigned)((li >> 1) & 7)) << 4);
      asm volatile("" : "+v"(xa[hf]), "+v"(xs_[hf]));
    }
    auto issue_w = [&](int s, int k) {
      int r = 3 * w + k; if (r > 7) r = 7;
      glds1<0>((unsigned)((s & 3) * 8192 + r * 1024), wq0 + (size_t)(s * 32 + 4 * r) * 8192, ((r >> 1) & 1) ? vWb : vWa);
    };
    auto issue_x = [&](int c, int k) {
      const int n = 6 * w + k;
      glds1<0>(XB + (unsigned)((c & 1) * XSZ + n * 1024), x0 + (size_t)(8 * n) * 1280 + c * 128, (k & 1) ? vXb : vXa);
    };
    auto wo_touch = [&]() {
#pragma unroll
      for (int e = 0; e < 3; ++e) {
        unsigned line = (unsigned)((blk & 255) * 160 + e * 64 + lane); if (e == 2 && lane >= 32) line = (unsigned)((blk & 255) * 160);
        glds4(line * 128u, P.wo[p], 68 * 1024);
      }
    };
    if (w == 3) {
      CODE_PREFETCH("k1", lane, 68 * 1024);
      wo_touch();
    } else {
      auto W3 = [&](int s) { issue_w(s, 0); issue_w(s, 1); issue_w(s, 2); };
      auto X6 = [&](int c) { issue_x(c, 0); issue_x(c, 1); issue_x(c, 2); issue_x(c, 3); issue_x(c, 4); issue_x(c, 5); };
      W3(0); X6(0); W3(1); W3(2); X6(1); W3(3);
    }
    bf16x8 A[2][8], Bx[2][3];
    if (w < 3) waitv<15>();
    bar();
#pragma unroll
    for (int i = 0; i < 8; ++i) A[0][i] = lds_tr2(ta[i], ta[i] + 1024);
    Bx[0][0] = lds_b128(xa[0]); Bx[0][1] = lds_b128(xa[0] + 2048); Bx[0][2] = lds_b128(xs_[0]);
#pragma nounroll
    for (int it = 0; it < 5; ++it) {
      const bool last = (it == 4);
#pragma unroll
      for (int u = 0; u < 4; ++u) {
        const int s = 4 * it + u;
        const int n = (u + 1) & 3;
        if (!(last && u == 3)) {
          if (w < 3) {
            if (!last) { if (u & 1) waitv<6>(); else waitv<12>(); }
            else if (u == 0) waitv<12>();
            else if (u == 1) waitv<3>();
            else waitv<8>();
          }
          bar();
          const unsigned xo = (unsigned)(((u + 1) >> 1) & 1) * XSZ;
          const int hf = (u + 1) & 1;
          Bx[(u + 1) & 1][0] = lds_b128(xa[hf] + xo); Bx[(u + 1) & 1][1] = lds_b128(xa[hf] + xo + 2048); Bx[(u + 1) & 1][2] = lds_b128(xs_[hf] + xo);
        }
#pragma unroll
        for (int i = 0; i < 8; ++i) {
          if (!(last && u == 3)) { const unsigned a = ta[i] + n * 8192; A[(u + 1) & 1][i] = lds_tr2(a, a + 1024); }
          qa0[i] = mfma(A[u & 1][i], Bx[u & 1][0], qa0[i]);
          qa1[i] = mfma(A[u & 1][i], Bx[u & 1][1], qa1[i]);
          if (i < 2) qaS[i] = mfma(A[u & 1][i], Bx[u & 1][2], qaS[i]);
          if (!last) {
            if (w < 3) {
              if (u & 1) {
                if (i < 6) issue_x(((s - 1) >> 1) + 2, i);
                if (i >= 5) issue_w(s + 4, i - 5);
              } else {
                if (i == 1 || i == 3 || i == 5) issue_w(s + 4, i >> 1);
              }
            }
          } else if (u == 1) {
            issue_piece(i >> 2, (i >> 2) ? 2 * SLOT : 0, i & 3);
          }
          __builtin_amdgcn_sched_barrier(0);
        }
      }
    }
  }
  bf16x8 Q0[4], Q1[4], QS[4];
  {
    bar();
    issue_tile(2, SLOT);
    const unsigned own = 3 * SLOT + w * 4096, shr = 4 * SLOT;
    auto waddr = [&](unsigned base, int T) { return base + li * 256 + ((((2 * T + (g >> 1)) ^ li) & 15) << 4) + 8 * (g & 1); };
    auto raddr = [&](unsigned base, int ds) { return base + li * 256 + ((((4 * ds + g) ^ li) & 15) << 4); };
    auto pkq = [&](const f32x4& a) { u32x2 v = {pk2(a[0] * SM_C, a[1] * SM_C), pk2(a[2] * SM_C, a[3] * SM_C)}; return v; };
#pragma unroll
    for (int t = 0; t < 2; ++t) *(LAS u32x2*)(uintptr_t)waddr(shr, 2 * w + t) = pkq(qaS[t]);
#pragma unroll
    for (int i = 0; i < 8; ++i) *(LAS u32x2*)(uintptr_t)waddr(own, (i + 2 * w) & 7) = pkq(qa0[i]);
    waitl0();
#pragma unroll
    for (int ds = 0; ds < 4; ++ds) Q0[ds] = lds_b128(raddr(own, ds));
    waitl0();
#pragma unroll
    for (int i = 0; i < 8; ++i) *(LAS u32x2*)(uintptr_t)waddr(own, (i + 2 * w) & 7) = pkq(qa1[i]);
    waitl0();
#pragma unroll
    for (int ds = 0; ds < 4; ++ds) Q1[ds] = lds_b128(raddr(own, ds));
    bar();
#pragma unroll
    for (int ds = 0; ds < 4; ++ds) QS[ds] = lds_b128(raddr(shr, ds));
    bar();
    issue_tile(3, 3 * SLOT);
  }
  f32x4 O0[8], O1[8], OS[8];
#pragma unroll
  for (int i = 0; i < 8; ++i) { O0[i] = f32x4{0, 0, 0, 0}; O1[i] = f32x4{0, 0, 0, 0}; OS[i] = f32x4{0, 0, 0, 0}; }
  f32x4 L0a = {0, 0, 0, 0}, L1a = {0, 0, 0, 0}, LSa = {0, 0, 0, 0};
  f32x4 n0 = {0, 0, 0, 0}, n1 = {0, 0, 0, 0}, nS = {0, 0, 0, 0};
  unsigned ka[4];
#pragma unroll
  for (int ds = 0; ds < 4; ++ds) ka[ds] = krow * 256 + ((((unsigned)(4 * ds + g) ^ (unsigned)li) & 15) << 4);
  const s16x8 ones_s = {0x3F80, 0x3F80, 0x3F80, 0x3F80, 0x3F80, 0x3F80, 0x3F80, 0x3F80};
  const bf16x8 ones = __builtin_bit_cast(bf16x8, ones_s);

  {
    auto slot_of = [](unsigned pos) { return (pos == 1 ? 2u : pos == 2 ? 1u : pos) * (unsigned)SLOT; };
    unsigned pK = 0, pV = 1, pNK = 4;
    unsigned sK = slot_of(pK), sV = slot_of(pV), sNK = slot_of(pNK);
    const unsigned cS = (unsigned)(w & 1) * 8192;
#pragma nounroll
    for (int it = 0; it < 8; ++it) {
      if (it < 7) waitv<12>(); else waitv<4>();
      bar();
      f32x4 S0[4], S1[4], SS;
      {
        unsigned kb[4];
#pragma unroll
        for (int ds = 0; ds < 4; ++ds) kb[ds] = ka[ds] + sK;
        bf16x8 F[2][4];
#pragma unroll
        for (int ds = 0; ds < 4; ++ds) F[0][ds] = lds_b128(kb[ds]);
#pragma unroll
        for (int kt = 0; kt < 4; ++kt) {
          if (kt < 3) {
#pragma unroll
            for (int ds = 0; ds < 4; ++ds) F[(kt + 1) & 1][ds] = lds_b128(kb[ds] + (32 * ((kt + 1) >> 1) + 4 * ((kt + 1) & 1)) * 256);
          }
          S0[kt] = mfma(F[kt & 1][0], Q0[0], n0);
          S1[kt] = mfma(F[kt & 1][0], Q1[0], n1);
#pragma unroll
          for (int ds = 1; ds < 4; ++ds) {
            S0[kt] = mfma(F[kt & 1][ds], Q0[ds], S0[kt]);
            S1[kt] = mfma(F[kt & 1][ds], Q1[ds], S1[kt]);
          }
          if (kt == w) {
            SS = mfma(F[kt & 1][0], QS[0], nS);
#pragma unroll
            for (int ds = 1; ds < 4; ++ds) SS = mfma(F[kt & 1][ds], QS[ds], SS);
          }
          if (it < 6) issue_piece(2 * it + 4, sNK, kt);
          __builtin_amdgcn_sched_barrier(0);
        }
      }
      bf16x8 P0[2], P1[2], PS;
      {
        asm volatile("s_nop 7\n\ts_nop 3" : "+v"(S0[3]), "+v"(S1[3]), "+v"(SS), "+v"(S0[2]), "+v"(S1[2]), "+v"(S0[1]), "+v"(S1[1]), "+v"(S0[0]), "+v"(S1[0]));
        const float l0_ = max8(S0[0], S0[1]), l1_ = max8(S1[0], S1[1]);
        float mx0 = xmax16_32(max3(max8(S0[2], S0[3]), l0_, l0_));
        float mx1 = xmax16_32(max3(max8(S1[2], S1[3]), l1_, l1_));
        float mxS = xmax16_32(max3(max3(SS[0], SS[1], SS[2]), SS[3], SS[3]));
        if (it == 0 || __any(max3(mx0, mx1, mxS) > SM_THR)) {
          const float d0 = (it == 0) ? mx0 : fmaxf(mx0, 0.f), d1 = (it == 0) ? mx1 : fmaxf(mx1, 0.f), dS = (it == 0) ? mxS : fmaxf(mxS, 0.f);
          n0 -= d0; n1 -= d1; nS -= dS; SS -= dS;
#pragma unroll
          for (int kt = 0; kt < 4; ++kt) { S0[kt] -= d0; S1[kt] -= d1; }
          if (it != 0) {
            const float a0 = __builtin_amdgcn_exp2f(-d0), a1 = __builtin_amdgcn_exp2f(-d1), aS = __builtin_amdgcn_exp2f(-dS);
#pragma unroll
            for (int dt = 0; dt < 8; ++dt) { O0[dt] *= a0; O1[dt] *= a1; OS[dt] *= aS; }
            L0a *= a0; L1a *= a1; LSa *= aS;
          }
        }
#pragma unroll
        for (int kt = 0; kt < 4; ++kt)
#pragma unroll
          for (int r = 0; r < 4; ++r) { S0[kt][r] = __builtin_amdgcn_exp2f(S0[kt][r]); S1[kt][r] = __builtin_amdgcn_exp2f(S1[kt][r]); }
#pragma unroll
        for (int r = 0; r < 4; ++r) SS[r] = __builtin_amdgcn_exp2f(SS[r]);
#pragma unroll
        for (int c = 0; c < 2; ++c) {
          u32x4 u = {pk2(S0[2 * c][0], S0[2 * c][1]), pk2(S0[2 * c][2], S0[2 * c][3]), pk2(S0[2 * c + 1][0], S0[2 * c + 1][1]), pk2(S0[2 * c + 1][2], S0[2 * c + 1][3])};
          P0[c] = __builtin_bit_cast(bf16x8, u);
          u32x4 v = {pk2(S1[2 * c][0], S1[2 * c][1]), pk2(S1[2 * c][2], S1[2 * c][3]), pk2(S1[2 * c + 1][0], S1[2 * c + 1][1]), pk2(S1[2 * c + 1][2], S1[2 * c + 1][3])};
          P1[c] = __builtin_bit_cast(bf16x8, v);
        }
        const unsigned a = pk2(SS[0], SS[1]), bb = pk2(SS[2], SS[3]);
        const bool odd = (w & 1) != 0;
        u32x4 u = {odd ? 0u : a, odd ? 0u : bb, odd ? a : 0u, odd ? bb : 0u};
        PS = __builtin_bit_cast(bf16x8, u);
      }
      L0a = mfma(ones, P0[0], L0a); L1a = mfma(ones, P1[0], L1a);
      L0a = mfma(ones, P0[1], L0a); L1a = mfma(ones, P1[1], L1a);
      LSa = mfma(ones, PS, LSa);
      if (it < 6) waitv<12>(); else if (it == 6) waitv<8>(); else waitv<0>();
      bar();
      const unsigned vb = tr0 + sV;
      {
        bf16x8 G[3][2];
#pragma unroll
        for (int dt = 0; dt < 2; ++dt)
#pragma unroll
          for (int c = 0; c < 2; ++c) { const unsigned a = (vb ^ (unsigned)(dt << 5)) + c * 8192; G[dt][c] = lds_tr2(a, a + 1024); }
#pragma unroll
        for (int dt = 0; dt < 8; ++dt) {
          if (dt + 2 < 8) {
#pragma unroll
            for (int c = 0; c < 2; ++c) { const unsigned a = (vb ^ (unsigned)((dt + 2) << 5)) + c * 8192; G[(dt + 2) % 3][c] = lds_tr2(a, a + 1024); }
          }
          O0[dt] = mfma(G[dt % 3][0], P0[0], O0[dt]);
          O1[dt] = mfma(G[dt % 3][0], P1[0], O1[dt]);
          O0[dt] = mfma(G[dt % 3][1], P0[1], O0[dt]);
          O1[dt] = mfma(G[dt % 3][1], P1[1], O1[dt]);
          if (w < 2) OS[dt] = mfma(G[dt % 3][0], PS, OS[dt]);
          else OS[dt] = mfma(G[dt % 3][1], PS, OS[dt]);
          if ((dt & 1) && it < 6) issue_piece(2 * it + 5, sK, dt >> 1);
          __builtin_amdgcn_sched_barrier(0);
        }
      }
      sNK = sV;
      pK += 2; if (pK >= 5) pK -= 5;
      pV += 2; if (pV >= 5) pV -= 5;
      sK = slot_of(pK); sV = slot_of(pV);
    }
  }
  char* ob = P.o + (size_t)((p * NB + b) * NH + h) * (9 * 4096) + lane * 16;
  {
    const unsigned so = SLOT + w * 8192 + lane * 16;
#pragma unroll
    for (int dt = 0; dt < 8; ++dt) *(LAS f32x4*)(uintptr_t)(so + dt * 1024) = OS[dt];
    if (g == 0) {
      f32x2 ml = {-nS[0], LSa[0]};
      *(LAS f32x2*)(uintptr_t)(3 * SLOT + (w * 16 + li) * 8) = ml;
    }
  }
  {
    const float i0 = 1.0f / L0a[0], i1 = 1.0f / L1a[0];
    char* r0 = ob + (size_t)(2 * w) * 4096;
#pragma unroll
    for (int ds = 0; ds < 4; ++ds) {
      u32x4 v0 = {pk2(O0[2 * ds][0] * i0, O0[2 * ds][1] * i0), pk2(O0[2 * ds][2] * i0, O0[2 * ds][3] * i0),
                  pk2(O0[2 * ds + 1][0] * i0, O0[2 * ds + 1][1] * i0), pk2(O0[2 * ds + 1][2] * i0, O0[2 * ds + 1][3] * i0)};
      u32x4 v1 = {pk2(O1[2 * ds][0] * i1, O1[2 * ds][1] * i1), pk2(O1[2 * ds][2] * i1, O1[2 * ds][3] * i1),
                  pk2(O1[2 * ds + 1][0] * i1, O1[2 * ds + 1][1] * i1), pk2(O1[2 * ds + 1][2] * i1, O1[2 * ds + 1][3] * i1)};
      *(u32x4*)(r0 + ds * 1024) = v0;
      *(u32x4*)(r0 + 4096 + ds * 1024) = v1;
    }
  }
  bar();
  {
    float mm[4], ll[4];
#pragma unroll
    for (int ww = 0; ww < 4; ++ww) {
      f32x2 ml = *(const LAS f32x2*)(uintptr_t)(3 * SLOT + (ww * 16 + li) * 8);
      mm[ww] = ml[0]; ll[ww] = ml[1];
    }
    const float M = fmaxf(fmaxf(mm[0], mm[1]), fmaxf(mm[2], mm[3]));
    float sc[4], L = 0.f;
#pragma unroll
    for (int ww = 0; ww < 4; ++ww) { sc[ww] = __builtin_amdgcn_exp2f(mm[ww] - M); L += sc[ww] * ll[ww]; }
    const float inv = 1.0f / L;
    f32x4 acc[2] = {f32x4{0, 0, 0, 0}, f32x4{0, 0, 0, 0}};
#pragma unroll
    for (int t = 0; t < 2; ++t)
#pragma unroll
      for (int ww = 0; ww < 4; ++ww) {
        const f32x4 v = *(const LAS f32x4*)(uintptr_t)(SLOT + ww * 8192 + (2 * w + t) * 1024 + lane * 16);
        acc[t] += v * sc[ww];
      }
    u32x4 v = {pk2(acc[0][0] * inv, acc[0][1] * inv), pk2(acc[0][2] * inv, acc[0][3] * inv),
               pk2(acc[1][0] * inv, acc[1][1] * inv), pk2(acc[1][2] * inv, acc[1][3] * inv)};
    *(u32x4*)(ob + (size_t)8 * 4096 + w * 1024) = v;
  }
  CODE_END("k1");
}

constexpr int OP_STAGE = 4096 + 6144;
constexpr int OP_WAVE = 4 * OP_STAGE;
constexpr int K2_LDS = 4 * OP_WAVE;

__global__ __launch_bounds__(512, 2) void k_oproj(AttnPtrs P) {
  extern __shared__ __attribute__((aligned(16))) char smem[];
  const int tid = threadIdx.x, lane = tid & 63;
  const int w8 = __builtin_amdgcn_readfirstlane(tid >> 6);
  const int w = w8 & 3;
  const int li = lane & 15, g = lane >> 4;
  const int blk = blockIdx.x;
  const int xcd = blk & 7, jj = blk >> 3;
  const int p = xcd >> 2, mt0 = 3 * (xcd & 3) + jj / 10, nt0 = jj % 10;
  const int m0 = mt0 * 96, n0 = nt0 * 64;
  const unsigned WB = (unsigned)(w * OP_WAVE);

  if (w8 >= 4) {
    const char* wo = P.wo[p] + (size_t)(w * 1024) * (DM * 2) + n0 * 2 - 4096;
    const char* ob = P.o + (size_t)(p * NB * NH + 8 * w) * 9 * 4096 - 8192;
    unsigned voffB[6];
#pragma unroll
    for (int i = 0; i < 6; ++i) {
      const int t = mt0 * 6 + i, b = t / 9, qt = t % 9;
      voffB[i] = 8192u - (unsigned)((i & 3) * 1024) + (unsigned)((b * NH * 9 + qt) * 4096 + lane * 16);
    }
    const int r8 = lane >> 3, c8 = lane & 7;
    unsigned voffW[4];
#pragma unroll
    for (int i = 0; i < 4; ++i)
      voffW[i] = 4096u - (unsigned)(i * 1024) + (unsigned)((8 * i + r8) * (DM * 2)) + (((((unsigned)(c8 >> 1)) ^ (unsigned)((lane >> 4) & 3)) << 1 | (c8 & 1)) * 16);
    auto issue = [&](int s) {
      const unsigned d = WB + (s & 3) * OP_STAGE;
      glds_quad(d, wo + (size_t)s * 32 * (DM * 2), voffW[0], voffW[1], voffW[2], voffW[3]);
      const char* os = ob + (size_t)(s >> 2) * (9 * 4096) + (s & 3) * 1024;
      glds_quad(d + 4096, os, voffB[0], voffB[1], voffB[2], voffB[3]);
      glds_duo(d + 8192, os, voffB[4], voffB[5]);
    };
    issue(0); issue(1); issue(2); issue(3);
    waitv<30>();
    __builtin_amdgcn_s_barrier();
#pragma nounroll
    for (int t = 0; t < 31; ++t) {
      if (t < 29) waitv<20>(); else if (t == 29) waitv<10>(); else waitv<0>();
      __builtin_amdgcn_s_barrier();
      if (t + 4 < 32) issue(t + 4);
    }
    __builtin_amdgcn_s_barrier();
    return;
  }
  const int q4 = li >> 2, p4 = lane & 3;
  const unsigned rr = (unsigned)(4 * g + q4);
  unsigned ta[4];
#pragma unroll
  for (int T = 0; T < 4; ++T) { ta[T] = WB + rr * 128 + 8 * p4 + ((((unsigned)T) ^ ((rr >> 1) & 3)) << 5); asm volatile("" : "+v"(ta[T])); }
  unsigned ba = WB + 4096 + lane * 16;
  asm volatile("" : "+v"(ba));
  f32x4 acc[4][6];
#pragma unroll
  for (int T = 0; T < 4; ++T)
#pragma unroll
    for (int mt = 0; mt < 6; ++mt) acc[T][mt] = f32x4{0, 0, 0, 0};
  bf16x8 af[2][4], bf[2][6];
  bar();
#pragma unroll
  for (int T = 0; T < 4; ++T) af[0][T] = lds_tr2(ta[T], ta[T] + 2048);
#pragma unroll
  for (int mt = 0; mt < 6; ++mt) bf[0][mt] = lds_b128(ba + mt * 1024);
#pragma nounroll
  for (int hh = 0; hh < 8; ++hh) {
#pragma unroll
    for (int u = 0; u < 4; ++u) {
      const int n = (u + 1) & 3;
      const bool more = !(hh == 7 && u == 3);
      if (more) bar();
#pragma unroll
      for (int T = 0; T < 4; ++T) {
#pragma unroll
        for (int mt = 0; mt < 6; ++mt) {
          acc[T][mt] = mfma(af[u & 1][T], bf[u & 1][mt], acc[T][mt]);
          if (more) {
            if (mt == 0) { const unsigned a = ta[T] + n * OP_STAGE; af[(u + 1) & 1][T] = lds_tr2(a, a + 2048); }
            if (mt == 2 && T < 3) { bf[(u + 1) & 1][2 * T] = lds_b128(ba + n * OP_STAGE + (2 * T) * 1024); }
            if (mt == 4 && T < 3) { bf[(u + 1) & 1][2 * T + 1] = lds_b128(ba + n * OP_STAGE + (2 * T + 1) * 1024); }
          }
          if (mt & 1) __builtin_amdgcn_sched_barrier(0);
        }
      }
    }
  }
#pragma unroll
  for (int T = 0; T < 4; ++T)
#pragma unroll
    for (int mt = 0; mt < 6; ++mt) *(LAS f32x4*)(uintptr_t)(WB + (T * 6 + mt) * 1024 + lane * 16) = acc[T][mt];
  bar();
  char* outp = P.out + ((size_t)m0 * (2 * DM) + p * DM + n0) * 2;
#pragma unroll
  for (int t = 0; t < 6; ++t) {
    const int id = w * 6 + t, T = w, mt = t;
    f32x4 s = {0, 0, 0, 0};
#pragma unroll
    for (int ww = 0; ww < 4; ++ww) s += *(const LAS f32x4*)(uintptr_t)(ww * OP_WAVE + id * 1024 + lane * 16);
    u32x2 v = {pk2(s[0], s[1]), pk2(s[2], s[3])};
    *(u32x2*)(outp + (size_t)(16 * mt + li) * (2 * DM * 2) + (16 * T + 4 * g) * 2) = v;
  }
}

extern "C" void kernel_launch(void* const* d_in, const int* in_sizes, int n_in, void* d_out, int out_size, void* d_ws,
                              size_t ws_size, hipStream_t stream) {
  AttnPtrs P;
  for (int p = 0; p < 2; ++p) {
    P.x[p] = (const char*)d_in[5 * p + 0];
    P.k[p] = (const char*)d_in[5 * p + 1];
    P.v[p] = (const char*)d_in[5 * p + 2];
    P.wq[p] = (const char*)d_in[5 * p + 3];
    P.wo[p] = (const char*)d_in[5 * p + 4];
  }
  P.o = (char*)d_ws;
  P.out = (char*)d_out;
  (void)hipFuncSetAttribute((const void*)k_attn, hipFuncAttributeMaxDynamicSharedMemorySize, K1_LDS);
  (void)hipFuncSetAttribute((const void*)k_oproj, hipFuncAttributeMaxDynamicSharedMemorySize, K2_LDS);
  k_attn<<<512, 256, K1_LDS, stream>>>(P);
  k_oproj<<<240, 512, K2_LDS, stream>>>(P);
}
```
